```python
import torch

def _softmax(x, axis=-1):
    return torch.softmax(x, dim=axis)
def _gelu(x):
    return torch.nn.functional.gelu(x, approximate="tanh")
def _silu(x):
    return torch.nn.functional.silu(x)
def _sigmoid(x):
    return torch.sigmoid(x)
def _layernorm(x, axis=-1, eps=1e-5):
    m = x.mean(dim=axis, keepdim=True)
    v = x.var(dim=axis, keepdim=True, unbiased=False)
    return (x - m) / torch.sqrt(v + eps)
def _scatter_add(tgt, idx, src, axis=0):
    return tgt.clone().index_add_(axis, idx, src)
def _segment_sum(src, seg, n):
    out = torch.zeros((n,) + src.shape[1:], dtype=src.dtype)
    return out.index_add_(0, seg, src)
def _pad(x, pw):
    flat = tuple(v for pair in reversed(pw) for v in pair)
    return torch.nn.functional.pad(x, flat)

kernel_name = 'parallel_sum__mha_block_cached__glu_gated_swiglu__decode'

def setup_inputs():

    _w = (lambda a: (lambda t: t.to(torch.bfloat16) if t.is_floating_point() else t)(torch.as_tensor(a)))
    def _A_setup():
        B, Sq, D = 2, 1, 512
        H, Sk, Dh = 32, 16384, 128
        return {
            "x": _w((lambda *s: torch.randn(*s, dtype=torch.bfloat16) * 0.05)(B, Sq, D)),
            "k_cache": _w((lambda *s: torch.randn(*s, dtype=torch.bfloat16) * 1.6)(B, H, Sk, Dh)),
            "v_cache": _w((lambda *s: torch.randn(*s, dtype=torch.bfloat16) * 0.8)(B, H, Sk, Dh)),
            "wq": _w((lambda *s: torch.randn(*s, dtype=torch.bfloat16) * 1.6)(D, H * Dh) * (D ** -0.5)),
            "wo": _w((lambda *s: torch.randn(*s, dtype=torch.bfloat16) * 0.8)(H * Dh, D) * ((H * Dh) ** -0.5)),
        }
    _A_in = _A_setup()

    def _B_setup():
        B, S, D, F = 2, 1, 512, 1792
        return {
            "x": _w((lambda *s: torch.randn(*s, dtype=torch.bfloat16) * 0.05)(B, S, D)),
            "w1": _w((lambda *s: torch.randn(*s, dtype=torch.bfloat16) * 0.4)(D, F) * (2.0 / D) ** 0.5),
            "w3": _w((lambda *s: torch.randn(*s, dtype=torch.bfloat16) * 0.4)(D, F) * (2.0 / D) ** 0.5),
            "w2": _w((lambda *s: torch.randn(*s, dtype=torch.bfloat16) * 0.4)(F, D) * (2.0 / F) ** 0.5),
        }
    _B_in = _B_setup()

    return {
        "A_x": _A_in["x"],
        "A_k_cache": _A_in["k_cache"],
        "A_v_cache": _A_in["v_cache"],
        "A_wq": _A_in["wq"],
        "A_wo": _A_in["wo"],
        "B_x": _B_in["x"],
        "B_w1": _B_in["w1"],
        "B_w3": _B_in["w3"],
        "B_w2": _B_in["w2"],
    }

def reference(A_x, A_k_cache, A_v_cache, A_wq, A_wo, B_x, B_w1, B_w3, B_w2):
    def _A_ref(x, k_cache, v_cache, wq, wo):
        B, Sq, D = x.shape
        H, Dh = 32, 128
        q = (lambda x, *d: x.permute(*d))((x @ wq).reshape(B, Sq, H, Dh), 0, 2, 1, 3)
        scores = torch.einsum("bhqd,bhkd->bhqk", q, k_cache) * (Dh ** -0.5)
        p = _softmax(scores, axis=-1)
        o = torch.einsum("bhqk,bhkd->bhqd", p, v_cache)
        return (lambda x, *d: x.permute(*d))(o, 0, 2, 1, 3).reshape(B, Sq, H * Dh) @ wo

    def _B_ref(x, w1, w3, w2):
        gate = torch.einsum("bsd,df->bsf", x, w1)
        up = torch.einsum("bsd,df->bsf", x, w3)
        h = _silu(gate) * up
        out = torch.einsum("bsf,fd->bsd", h, w2)
        return out

    x = A_x
    xn = _layernorm(x, axis=-1)
    return x + _A_ref(xn, A_k_cache, A_v_cache, A_wq, A_wo) + _B_ref(xn, B_w1, B_w3, B_w2)
```

```cpp
#include <hip/hip_runtime.h>
#include <hip/hip_bf16.h>
#include <cstdint>

#define WAUX 0
#define MIMIC_SCORE_ROUND 0

namespace {

constexpr int NB = 2, DM = 512, NH = 32, SK = 16384, DH = 128, FF = 1792;
constexpr int NSPLIT = 4, KEYS_WG = SK / NSPLIT;
constexpr int NSW = 8;
constexpr int NTHR = (NSW + 1) * 64;
constexpr int NWG = NB * NH * NSPLIT;
constexpr int GL = 4;
constexpr int RG = 4;
constexpr int RSL = GL * RG;
constexpr int NGRP = KEYS_WG / (NSW * GL * 4);
constexpr int NREV = NGRP / RG;
constexpr int FSL = 64, NFW = FF / FSL;

constexpr int WS_Y = 0;
constexpr int WS_ML = WS_Y + NWG * DM;
constexpr int WS_F = WS_ML + NWG * 2;

typedef unsigned u32x4 __attribute__((ext_vector_type(4)));
typedef float f32x4 __attribute__((ext_vector_type(4)));
typedef __bf16 bf16x2 __attribute__((ext_vector_type(2)));
#define GAS __attribute__((address_space(1)))

__device__ __forceinline__ float dot2(unsigned a, unsigned b, float acc) {
  return __builtin_amdgcn_fdot2_f32_bf16(__builtin_bit_cast(bf16x2, a), __builtin_bit_cast(bf16x2, b), acc, false);
}
__device__ __forceinline__ float blo(unsigned u) { return __builtin_bit_cast(float, u << 16); }
__device__ __forceinline__ float bhi(unsigned u) { return __builtin_bit_cast(float, u & 0xffff0000u); }
__device__ __forceinline__ float rbf(float f) { return (float)(__bf16)f; }
__device__ __forceinline__ unsigned pack2(float lo, float hi) {
  bf16x2 v; v.x = (__bf16)lo; v.y = (__bf16)hi; return __builtin_bit_cast(unsigned, v);
}
template <int CTRL> __device__ __forceinline__ float dpp(float x) {
  return __builtin_bit_cast(float, __builtin_amdgcn_mov_dpp(__builtin_bit_cast(int, x), CTRL, 0xf, 0xf, true));
}
constexpr int XOR1 = 0xB1, XOR2 = 0x4E, HMIR = 0x141, ROR8 = 0x128;
__device__ __forceinline__ float row16_sum(float x) {
  x += dpp<XOR1>(x); x += dpp<XOR2>(x); x += dpp<HMIR>(x); x += dpp<ROR8>(x); return x;
}
__device__ __forceinline__ float x16_sum(float x) {
  auto s = __builtin_amdgcn_permlane16_swap(__float_as_uint(x), __float_as_uint(x), false, false);
  return __uint_as_float(s[0]) + __uint_as_float(s[1]);
}
__device__ __forceinline__ float x32_sum(float x) {
  auto s = __builtin_amdgcn_permlane32_swap(__float_as_uint(x), __float_as_uint(x), false, false);
  return __uint_as_float(s[0]) + __uint_as_float(s[1]);
}
__device__ __forceinline__ float xrow_sum(float x) { return x32_sum(x16_sum(x)); }
__device__ __forceinline__ float xrow_max(float x) {
  auto s = __builtin_amdgcn_permlane16_swap(__float_as_uint(x), __float_as_uint(x), false, false);
  x = fmaxf(__uint_as_float(s[0]), __uint_as_float(s[1]));
  auto t = __builtin_amdgcn_permlane32_swap(__float_as_uint(x), __float_as_uint(x), false, false);
  return fmaxf(__uint_as_float(t[0]), __uint_as_float(t[1]));
}
__device__ __forceinline__ float wave_sum(float x) { return xrow_sum(row16_sum(x)); }
__device__ __forceinline__ u32x4 ldnt(const GAS u32x4* p) { return __builtin_nontemporal_load(p); }
__device__ __forceinline__ u32x4 ldg(const GAS u32x4* p) { return *p; }
typedef __amdgpu_buffer_rsrc_t rsrc_t;
__device__ __forceinline__ rsrc_t mkrsrc(const void* p, int bytes) { return __builtin_amdgcn_make_buffer_rsrc((void*)p, 0, bytes, 0x00020000); }
template <int AUX> __device__ __forceinline__ u32x4 bld(rsrc_t r, int voff, int soff) { return __builtin_amdgcn_raw_buffer_load_b128(r, voff, soff, AUX); }
#define PIN4(a) asm volatile("" : "+v"(a[0]), "+v"(a[1]), "+v"(a[2]), "+v"(a[3])::"memory"); __builtin_amdgcn_sched_barrier(0)
#define PIN8(a) asm volatile("" : "+v"(a[0]), "+v"(a[1]), "+v"(a[2]), "+v"(a[3]), "+v"(a[4]), "+v"(a[5]), "+v"(a[6]), "+v"(a[7])::"memory"); __builtin_amdgcn_sched_barrier(0)

__device__ __forceinline__ void ln_stats(u32x4 xa, float& mean_b, float& sd_b) {
  float e[8];
#pragma unroll
  for (int k = 0; k < 4; ++k) { e[2 * k] = blo(xa[k]); e[2 * k + 1] = bhi(xa[k]); }
  float s = 0.f;
#pragma unroll
  for (int k = 0; k < 8; ++k) s += e[k];
  const float mean = wave_sum(s) * (1.0f / DM);
  float q = 0.f;
#pragma unroll
  for (int k = 0; k < 8; ++k) { const float d = e[k] - mean; q = fmaf(d, d, q); }
  const float var = wave_sum(q) * (1.0f / DM);
  mean_b = rbf(mean);
  sd_b = rbf(sqrtf(rbf(rbf(var) + 1e-5f)));
}
__device__ __forceinline__ float ln_apply(float x, float mean_b, float sd_b) { return rbf(rbf(x - mean_b) / sd_b); }

constexpr int L_QP = 0;
constexpr int L_ML = L_QP + NSW * DH;
constexpr int L_O = L_ML + 32;
constexpr int L_XN = L_O + NSW * DH;
constexpr int L_H = L_XN + 2 * DM;
constexpr int L_TOT = L_H + 2 * FSL;

__global__ void __launch_bounds__(NTHR) attn_kernel(const unsigned short* __restrict__ x, const unsigned short* __restrict__ kc,
                                                    const unsigned short* __restrict__ vc, const unsigned short* __restrict__ wq,
                                                    const unsigned short* __restrict__ wo, const unsigned short* __restrict__ w1,
                                                    const unsigned short* __restrict__ w3, const unsigned short* __restrict__ w2,
                                                    float* __restrict__ ws) {
  __shared__ __attribute__((aligned(16))) float lds[L_TOT];
  const int bid = blockIdx.x;
  const int tid = threadIdx.x, lane = tid & 63, w = __builtin_amdgcn_readfirstlane(tid >> 6);
  const int j = bid >> 3, h = (bid & 7) * 4 + (j >> 3), b = (j >> 2) & 1, s = j & 3;

  if (w == NSW) {
    if (bid >= NFW) return;
    const int f0 = bid * FSL;
    const int rr = lane >> 3, cc = lane & 7;
    const u32x4 xa0 = ldg((const GAS u32x4*)x + lane), xa1 = ldg((const GAS u32x4*)x + 64 + lane);
    __builtin_amdgcn_s_barrier();
    float mb0, sd0, mb1, sd1;
    ln_stats(xa0, mb0, sd0); ln_stats(xa1, mb1, sd1);
    float* xn = lds + L_XN;
#pragma unroll
    for (int k = 0; k < 4; ++k) {
      xn[8 * lane + 2 * k] = ln_apply(blo(xa0[k]), mb0, sd0); xn[8 * lane + 2 * k + 1] = ln_apply(bhi(xa0[k]), mb0, sd0);
      xn[DM + 8 * lane + 2 * k] = ln_apply(blo(xa1[k]), mb1, sd1); xn[DM + 8 * lane + 2 * k + 1] = ln_apply(bhi(xa1[k]), mb1, sd1);
    }
    float g[2][8], u[2][8];
#pragma unroll
    for (int k = 0; k < 8; ++k) { g[0][k] = g[1][k] = u[0][k] = u[1][k] = 0.f; }
    const GAS u32x4* p1 = (const GAS u32x4*)(w1 + (size_t)rr * FF + f0 + 8 * cc);
    const GAS u32x4* p3 = (const GAS u32x4*)(w3 + (size_t)rr * FF + f0 + 8 * cc);
    for (int it = 0; it < 8; ++it) {
      u32x4 a1[8], a3[8];
#pragma unroll
      for (int i = 0; i < 8; ++i) { a1[i] = ldnt(p1 + (size_t)(it * 8 + i) * FF); a3[i] = ldnt(p3 + (size_t)(it * 8 + i) * FF); }
#pragma unroll
      for (int i = 0; i < 8; ++i) {
        const int row = (it * 8 + i) * 8 + rr;
        const float x0 = xn[row], x1 = xn[DM + row];
#pragma unroll
        for (int k = 0; k < 4; ++k) {
          const float a = blo(a1[i][k]), c = bhi(a1[i][k]), d = blo(a3[i][k]), e = bhi(a3[i][k]);
          g[0][2 * k] = fmaf(x0, a, g[0][2 * k]); g[0][2 * k + 1] = fmaf(x0, c, g[0][2 * k + 1]);
          g[1][2 * k] = fmaf(x1, a, g[1][2 * k]); g[1][2 * k + 1] = fmaf(x1, c, g[1][2 * k + 1]);
          u[0][2 * k] = fmaf(x0, d, u[0][2 * k]); u[0][2 * k + 1] = fmaf(x0, e, u[0][2 * k + 1]);
          u[1][2 * k] = fmaf(x1, d, u[1][2 * k]); u[1][2 * k + 1] = fmaf(x1, e, u[1][2 * k + 1]);
        }
      }
    }
    float* hh = lds + L_H;
#pragma unroll
    for (int bb = 0; bb < 2; ++bb)
#pragma unroll
      for (int k = 0; k < 8; ++k) {
        float gg = g[bb][k], uu = u[bb][k];
        gg += dpp<ROR8>(gg); uu += dpp<ROR8>(uu);
        gg = rbf(xrow_sum(gg)); uu = rbf(xrow_sum(uu));
        const float si = rbf(gg / (1.0f + __expf(-gg)));
        if (rr == 0) hh[bb * FSL + 8 * cc + k] = rbf(si * uu);
      }
    float o0[8], o1[8];
#pragma unroll
    for (int k = 0; k < 8; ++k) { o0[k] = o1[k] = 0.f; }
    const GAS u32x4* p2 = (const GAS u32x4*)(w2 + (size_t)f0 * DM) + lane;
    for (int it = 0; it < 4; ++it) {
      u32x4 a2[16];
#pragma unroll
      for (int i = 0; i < 16; ++i) a2[i] = ldnt(p2 + (it * 16 + i) * 64);
#pragma unroll
      for (int i = 0; i < 16; ++i) {
        const float h0 = hh[it * 16 + i], h1 = hh[FSL + it * 16 + i];
#pragma unroll
        for (int k = 0; k < 4; ++k) {
          const float a = blo(a2[i][k]), c = bhi(a2[i][k]);
          o0[2 * k] = fmaf(h0, a, o0[2 * k]); o0[2 * k + 1] = fmaf(h0, c, o0[2 * k + 1]);
          o1[2 * k] = fmaf(h1, a, o1[2 * k]); o1[2 * k + 1] = fmaf(h1, c, o1[2 * k + 1]);
        }
      }
    }
    float* fo = ws + WS_F + (size_t)bid * 2 * DM + 8 * lane;
    *(f32x4*)(fo) = f32x4{o0[0], o0[1], o0[2], o0[3]}; *(f32x4*)(fo + 4) = f32x4{o0[4], o0[5], o0[6], o0[7]};
    *(f32x4*)(fo + DM) = f32x4{o1[0], o1[1], o1[2], o1[3]}; *(f32x4*)(fo + DM + 4) = f32x4{o1[4], o1[5], o1[6], o1[7]};
    return;
  }

  const int r = lane >> 4, c = lane & 15;
  u32x4 ring[2 * RSL];
  const int vo = lane * 16;
  const size_t kvbase = ((size_t)(b * NH + h) * SK + (size_t)s * KEYS_WG) * DH;
  const rsrc_t rk = mkrsrc(kc + kvbase, KEYS_WG * DH * 2), rv = mkrsrc(vc + kvbase, KEYS_WG * DH * 2);
  const rsrc_t rx = mkrsrc(x + b * DM, DM * 2);
  const rsrc_t rq = mkrsrc(wq + h * DH, (DM * NH * DH - h * DH) * 2);
  const rsrc_t ro = mkrsrc(wo + (size_t)h * DH * DM, DH * DM * 2);
  const u32x4 xa = bld<0>(rx, vo, 0);
  const u32x4 xb0 = bld<0>(rx, 32 * r, 128 * w), xb1 = bld<0>(rx, 32 * r + 16, 128 * w);
  {
    const int vq = r * (16 * NH * DH * 2) + c * 16;
#pragma unroll
    for (int i = 0; i < 8; ++i) ring[8 + i] = bld<WAUX>(rq, vq, (64 * w + i) * (NH * DH * 2));
#pragma unroll
    for (int i = 0; i < 8; ++i) ring[RSL + 8 + i] = bld<WAUX>(rq, vq, (64 * w + 8 + i) * (NH * DH * 2));
  }
  __builtin_amdgcn_sched_barrier(0);
  constexpr int GSTR = NSW * GL * 1024, OMASK = KEYS_WG * DH * 2 - 1;
  int so = w * (GL * 1024) + (s & 1) * (KEYS_WG * DH);
#define FILL_K(g, o) _Pragma("unroll") for (int i = 0; i < GL; ++i) ring[(g) * GL + i] = bld<2>(rk, vo + i * 1024, o); __builtin_amdgcn_sched_barrier(0);
#define FILL_V(g, o) _Pragma("unroll") for (int i = 0; i < GL; ++i) ring[RSL + (g) * GL + i] = bld<2>(rv, vo + i * 1024, o); __builtin_amdgcn_sched_barrier(0);
  FILL_K(0, so) FILL_V(0, so) FILL_K(1, so + GSTR) FILL_V(1, so + GSTR)
  float mb, sd;
  ln_stats(xa, mb, sd);
  float qa[8];
#pragma unroll
  for (int k = 0; k < 8; ++k) qa[k] = 0.f;
#pragma unroll
  for (int i = 0; i < 16; ++i) {
    const unsigned xw = i < 8 ? xb0[i >> 1] : xb1[(i - 8) >> 1];
    const float xn = ln_apply((i & 1) ? bhi(xw) : blo(xw), mb, sd);
    const u32x4 wv = i < 8 ? ring[8 + i] : ring[RSL + i];
#pragma unroll
    for (int k = 0; k < 4; ++k) { qa[2 * k] = fmaf(xn, blo(wv[k]), qa[2 * k]); qa[2 * k + 1] = fmaf(xn, bhi(wv[k]), qa[2 * k + 1]); }
  }
  PIN8(qa);
  FILL_K(2, so + 2 * GSTR) FILL_V(2, so + 2 * GSTR) FILL_K(3, so + 3 * GSTR) FILL_V(3, so + 3 * GSTR)
#pragma unroll
  for (int k = 0; k < 8; ++k) qa[k] = xrow_sum(qa[k]);
  if (r == 0) {
    *(f32x4*)(lds + L_QP + w * DH + 8 * c) = f32x4{qa[0], qa[1], qa[2], qa[3]};
    *(f32x4*)(lds + L_QP + w * DH + 8 * c + 4) = f32x4{qa[4], qa[5], qa[6], qa[7]};
  }
  asm volatile("s_waitcnt lgkmcnt(0)" ::: "memory");
  __builtin_amdgcn_s_barrier();
  unsigned qp[4];
  {
    f32x4 t0 = {0.f, 0.f, 0.f, 0.f}, t1 = {0.f, 0.f, 0.f, 0.f};
#pragma unroll
    for (int ww = 0; ww < NSW; ++ww) {
      t0 += *(const f32x4*)(lds + L_QP + ww * DH + 8 * c);
      t1 += *(const f32x4*)(lds + L_QP + ww * DH + 8 * c + 4);
    }
    qp[0] = pack2(t0[0], t0[1]); qp[1] = pack2(t0[2], t0[3]); qp[2] = pack2(t1[0], t1[1]); qp[3] = pack2(t1[2], t1[3]);
  }
  constexpr float SCALE = 0.088388347648318440f, LOG2E = 1.4426950408889634f;
  float m = -INFINITY, l = 0.f, acc[8];
#pragma unroll
  for (int k = 0; k < 8; ++k) acc[k] = 0.f;

#define GROUP_BODY(g, REFILL_K, REFILL_V)                                                            \
  {                                                                                                  \
    float sc[GL];                                                                                    \
    _Pragma("unroll") for (int i = 0; i < GL; ++i) {                                                 \
      float d = 0.f;                                                                                 \
      _Pragma("unroll") for (int k = 0; k < 4; ++k) d = dot2(ring[(g) * GL + i][k], qp[k], d);       \
      sc[i] = d;                                                                                     \
    }                                                                                                \
    PIN4(sc);                                                                                        \
    REFILL_K                                                                                         \
    float mn = m;                                                                                    \
    _Pragma("unroll") for (int i = 0; i < GL; ++i) {                                                 \
      float t = row16_sum(sc[i]);                                                                    \
      if (MIMIC_SCORE_ROUND) t = rbf(rbf(t) * SCALE) * LOG2E; else t *= SCALE * LOG2E;               \
      sc[i] = t; mn = fmaxf(mn, t);                                                                  \
    }                                                                                                \
    const float al = __builtin_amdgcn_exp2f(m - mn);                                                 \
    m = mn; l *= al;                                                                                 \
    _Pragma("unroll") for (int k = 0; k < 8; ++k) acc[k] *= al;                                      \
    _Pragma("unroll") for (int i = 0; i < GL; ++i) {                                                 \
      const float p = __builtin_amdgcn_exp2f(sc[i] - mn);                                            \
      l += p;                                                                                        \
      const u32x4 vv = ring[RSL + (g) * GL + i];                                                     \
      _Pragma("unroll") for (int k = 0; k < 4; ++k) {                                                \
        acc[2 * k] = fmaf(p, blo(vv[k]), acc[2 * k]); acc[2 * k + 1] = fmaf(p, bhi(vv[k]), acc[2 * k + 1]); \
      }                                                                                              \
      PIN8(acc);                                                                                     \
    }                                                                                                \
    REFILL_V                                                                                         \
  }

#pragma nounroll
  for (int rev = 0; rev < NREV - 1; ++rev) {
    so = (so + RG * GSTR) & OMASK;
    GROUP_BODY(0, FILL_K(0, so), FILL_V(0, so))
    GROUP_BODY(1, FILL_K(1, so + GSTR), FILL_V(1, so + GSTR))
    GROUP_BODY(2, FILL_K(2, so + 2 * GSTR), FILL_V(2, so + 2 * GSTR))
    GROUP_BODY(3, FILL_K(3, so + 3 * GSTR), FILL_V(3, so + 3 * GSTR))
  }
  const int vw = (lane >> 3) * (16 * DM * 2) + (lane & 7) * 16;
#define FILL_O(slot, q) _Pragma("unroll") for (int i = 0; i < GL; ++i) ring[(slot) + i] = bld<WAUX>(ro, vw + i * 1024, (q) * 4096 + w * 128); __builtin_amdgcn_sched_barrier(0);
  GROUP_BODY(0, FILL_O(0, 0), FILL_O(RSL, 1))
  GROUP_BODY(1, FILL_O(GL, 2), FILL_O(RSL + GL, 3))
  GROUP_BODY(2, , )
  GROUP_BODY(3, , )
#undef GROUP_BODY
  {
    const float M = xrow_max(m);
    const float f = __builtin_amdgcn_exp2f(m - M);
    l = xrow_sum(l * f);
#pragma unroll
    for (int k = 0; k < 8; ++k) acc[k] = xrow_sum(acc[k] * f);
    if (r == 0) {
      *(f32x4*)(lds + L_O + w * DH + 8 * c) = f32x4{acc[0], acc[1], acc[2], acc[3]};
      *(f32x4*)(lds + L_O + w * DH + 8 * c + 4) = f32x4{acc[4], acc[5], acc[6], acc[7]};
    }
    if (lane == 0) { lds[L_ML + 2 * w] = M; lds[L_ML + 2 * w + 1] = l; }
  }
  asm volatile("s_waitcnt lgkmcnt(0)" ::: "memory");
  __builtin_amdgcn_s_barrier();
  float M = -INFINITY;
#pragma unroll
  for (int ww = 0; ww < NSW; ++ww) M = fmaxf(M, lds[L_ML + 2 * ww]);
  float L = 0.f, o0 = 0.f, o1 = 0.f;
#pragma unroll
  for (int ww = 0; ww < NSW; ++ww) {
    const float f = __builtin_amdgcn_exp2f(lds[L_ML + 2 * ww] - M);
    L = fmaf(f, lds[L_ML + 2 * ww + 1], L);
    const float2 a = *(const float2*)(lds + L_O + ww * DH + 2 * lane);
    o0 = fmaf(f, a.x, o0); o1 = fmaf(f, a.y, o1);
  }
  float* om = lds + L_QP + w * DH;
  *(float2*)(om + 2 * lane) = make_float2(o0, o1);
  asm volatile("s_waitcnt lgkmcnt(0)" ::: "memory");
  float ov[16];
#pragma unroll
  for (int i = 0; i < 4; ++i) {
    const f32x4 t = *(const f32x4*)(om + 16 * (lane >> 3) + 4 * i);
    ov[4 * i] = t[0]; ov[4 * i + 1] = t[1]; ov[4 * i + 2] = t[2]; ov[4 * i + 3] = t[3];
  }
  float y[8];
#pragma unroll
  for (int k = 0; k < 8; ++k) y[k] = 0.f;
#pragma unroll
  for (int i = 0; i < 16; ++i)
#pragma unroll
    for (int k = 0; k < 4; ++k) {
      const unsigned wv = ring[((i >> 2) & 1) * RSL + (i >> 3) * GL + (i & 3)][k];
      y[2 * k] = fmaf(ov[i], blo(wv), y[2 * k]); y[2 * k + 1] = fmaf(ov[i], bhi(wv), y[2 * k + 1]);
    }
#pragma unroll
  for (int k = 0; k < 8; ++k) { float t = y[k]; t += dpp<ROR8>(t); y[k] = xrow_sum(t); }
  const int slot = (b * NH + h) * NSPLIT + s;
  if (lane < 8) {
    float* yo = ws + WS_Y + (size_t)slot * DM + 64 * w + 8 * lane;
    *(f32x4*)(yo) = f32x4{y[0], y[1], y[2], y[3]}; *(f32x4*)(yo + 4) = f32x4{y[4], y[5], y[6], y[7]};
  }
  if (tid == 0) *(float2*)(ws + WS_ML + 2 * slot) = make_float2(M, L);
}

__global__ void __launch_bounds__(256) combine_kernel(const unsigned short* __restrict__ x, const float* __restrict__ ws,
                                                      unsigned short* __restrict__ out) {
  __shared__ __attribute__((aligned(16))) float red[2][4][16];
  const int b = blockIdx.x >> 5, cg = blockIdx.x & 31;
  const int tid = threadIdx.x, lane = tid & 63, w = tid >> 6;
  const int q4 = tid & 3, pr = tid >> 2;
  const int col = cg * 16 + q4 * 4;
  const int p0 = b * 128 + pr, p1 = p0 + 64;
  const f32x4 y0 = *(const f32x4*)(ws + WS_Y + (size_t)p0 * DM + col);
  const f32x4 y1 = *(const f32x4*)(ws + WS_Y + (size_t)p1 * DM + col);
  const f32x4 ma0 = *(const f32x4*)(ws + WS_ML + 2 * (p0 & ~3)), ma1 = *(const f32x4*)(ws + WS_ML + 2 * (p0 & ~3) + 4);
  const f32x4 mb0 = *(const f32x4*)(ws + WS_ML + 2 * (p1 & ~3)), mb1 = *(const f32x4*)(ws + WS_ML + 2 * (p1 & ~3) + 4);
  f32x4 fv = {0.f, 0.f, 0.f, 0.f};
  if (pr < NFW) fv = *(const f32x4*)(ws + WS_F + (size_t)(pr * 2 + b) * DM + col);
  auto coef = [&](f32x4 a, f32x4 c, int sidx) {
    const float M = fmaxf(fmaxf(a[0], a[2]), fmaxf(c[0], c[2]));
    const float e0 = __builtin_amdgcn_exp2f(a[0] - M), e1 = __builtin_amdgcn_exp2f(a[2] - M);
    const float e2 = __builtin_amdgcn_exp2f(c[0] - M), e3 = __builtin_amdgcn_exp2f(c[2] - M);
    const float L = e0 * a[1] + e1 * a[3] + e2 * c[1] + e3 * c[3];
    const float e = sidx == 0 ? e0 : sidx == 1 ? e1 : sidx == 2 ? e2 : e3;
    return e / L;
  };
  const float c0 = coef(ma0, ma1, pr & 3), c1 = coef(mb0, mb1, pr & 3);
  float a[4], f[4];
#pragma unroll
  for (int k = 0; k < 4; ++k) {
    float t = c0 * y0[k] + c1 * y1[k], u = fv[k];
    t += dpp<0x124>(t); u += dpp<0x124>(u);
    t += dpp<ROR8>(t); u += dpp<ROR8>(u);
    a[k] = xrow_sum(t); f[k] = xrow_sum(u);
  }
  if (lane < 4) {
    *(f32x4*)(&red[0][w][lane * 4]) = f32x4{a[0], a[1], a[2], a[3]};
    *(f32x4*)(&red[1][w][lane * 4]) = f32x4{f[0], f[1], f[2], f[3]};
  }
  __syncthreads();
  if (tid < 16) {
    const float A = (red[0][0][tid] + red[0][1][tid]) + (red[0][2][tid] + red[0][3][tid]);
    const float F = (red[1][0][tid] + red[1][1][tid]) + (red[1][2][tid] + red[1][3][tid]);
    const int n = b * DM + cg * 16 + tid;
    const float xv = __builtin_bit_cast(float, (unsigned)x[n] << 16);
    const float r = rbf(xv + rbf(A)) + rbf(F);
    out[n] = __builtin_bit_cast(unsigned short, (__bf16)r);
  }
}

}

extern "C" void kernel_launch(void* const* d_in, const int* in_sizes, int n_in, void* d_out, int out_size, void* d_ws,
                              size_t ws_size, hipStream_t stream) {
  const unsigned short* x = (const unsigned short*)d_in[0];
  const unsigned short* kc = (const unsigned short*)d_in[1];
  const unsigned short* vc = (const unsigned short*)d_in[2];
  const unsigned short* wq = (const unsigned short*)d_in[3];
  const unsigned short* wo = (const unsigned short*)d_in[4];
  const unsigned short* w1 = (const unsigned short*)d_in[6];
  const unsigned short* w3 = (const unsigned short*)d_in[7];
  const unsigned short* w2 = (const unsigned short*)d_in[8];
  float* ws = (float*)d_ws;
  attn_kernel<<<NWG, NTHR, 0, stream>>>(x, kc, vc, wq, wo, w1, w3, w2, ws);
  combine_kernel<<<64, 256, 0, stream>>>(x, ws, (unsigned short*)d_out);
}
```

```cpp
#include <hip/hip_runtime.h>
#include <hip/hip_bf16.h>
#include <cstdint>

#define ABL_WQ 0
#define ABL_WO 0
#define ABL_FFN 0
#define MIMIC_SCORE_ROUND 0
#define WQ_EARLY 0
#define WO_SLEEP 100
#define FSH 2

namespace {

constexpr int NB = 2, DM = 512, NH = 32, SK = 16384, DH = 128, FF = 1792;
constexpr int NSPLIT = 4;
constexpr int NSW = 4, NTHR = 512;
constexpr int NWG = NB * NH * NSPLIT;
constexpr int GL = 4;
#define RGV 4
constexpr int RG = RGV;
constexpr int RSL = GL * RG;
constexpr int STEPK = NSW * GL * 4, STEPB = STEPK * DH * 2;
constexpr int NSTEP = SK / NSPLIT / STEPK;
constexpr int FSL = 64, NFW = FF / FSL;

constexpr int WS_Y = 0;
constexpr int WS_ML = WS_Y + NWG * DM;
constexpr int WS_F = WS_ML + NWG * 2;

typedef unsigned u32x4 __attribute__((ext_vector_type(4)));
typedef float f32x4 __attribute__((ext_vector_type(4)));
typedef float f32x2 __attribute__((ext_vector_type(2)));
typedef __bf16 bf16x2 __attribute__((ext_vector_type(2)));
typedef __bf16 bf16x8 __attribute__((ext_vector_type(8)));
typedef unsigned short u16;
#define GAS __attribute__((address_space(1)))
#define LAS __attribute__((address_space(3)))

__device__ __forceinline__ float dot2(unsigned a, unsigned b, float acc) {
  return __builtin_amdgcn_fdot2_f32_bf16(__builtin_bit_cast(bf16x2, a), __builtin_bit_cast(bf16x2, b), acc, false);
}
__device__ __forceinline__ float blo(unsigned u) { return __builtin_bit_cast(float, u << 16); }
__device__ __forceinline__ float bhi(unsigned u) { return __builtin_bit_cast(float, u & 0xffff0000u); }
__device__ __forceinline__ float rbf(float f) { return (float)(__bf16)f; }
__device__ __forceinline__ unsigned pack2(float lo, float hi) {
  bf16x2 v; v.x = (__bf16)lo; v.y = (__bf16)hi; return __builtin_bit_cast(unsigned, v);
}
template <int CTRL> __device__ __forceinline__ float dpp(float x) {
  return __builtin_bit_cast(float, __builtin_amdgcn_mov_dpp(__builtin_bit_cast(int, x), CTRL, 0xf, 0xf, true));
}
constexpr int XOR1 = 0xB1, XOR2 = 0x4E, HMIR = 0x141, ROR8 = 0x128, ROR4 = 0x124;
__device__ __forceinline__ float row16_sum(float x) {
  x += dpp<XOR1>(x); x += dpp<XOR2>(x); x += dpp<HMIR>(x); x += dpp<ROR8>(x); return x;
}
__device__ __forceinline__ float t16_sum(float a, float b) {
  auto s = __builtin_amdgcn_permlane16_swap(__float_as_uint(a), __float_as_uint(b), false, false);
  return __uint_as_float(s[0]) + __uint_as_float(s[1]);
}
__device__ __forceinline__ float t32_sum(float a, float b) {
  auto s = __builtin_amdgcn_permlane32_swap(__float_as_uint(a), __float_as_uint(b), false, false);
  return __uint_as_float(s[0]) + __uint_as_float(s[1]);
}
__device__ __forceinline__ float xrow_sum(float x) { x = t16_sum(x, x); return t32_sum(x, x); }
__device__ __forceinline__ float xrow_max(float x) {
  auto s = __builtin_amdgcn_permlane16_swap(__float_as_uint(x), __float_as_uint(x), false, false);
  x = fmaxf(__uint_as_float(s[0]), __uint_as_float(s[1]));
  auto t = __builtin_amdgcn_permlane32_swap(__float_as_uint(x), __float_as_uint(x), false, false);
  return fmaxf(__uint_as_float(t[0]), __uint_as_float(t[1]));
}
__device__ __forceinline__ float wave_sum(float x) { return xrow_sum(row16_sum(x)); }
__device__ __forceinline__ f32x2 xrow_tsum8(const float (&v)[8]) {
  const float a0 = t32_sum(v[0], v[4]), a1 = t32_sum(v[1], v[5]), a2 = t32_sum(v[2], v[6]), a3 = t32_sum(v[3], v[7]);
  return f32x2{t16_sum(a0, a2), t16_sum(a1, a3)};
}
typedef __amdgpu_buffer_rsrc_t rsrc_t;
__device__ __forceinline__ rsrc_t mkrsrc(const void* p, int bytes) { return __builtin_amdgcn_make_buffer_rsrc((void*)p, 0, bytes, 0x00020000); }
__device__ __forceinline__ u32x4 bld(rsrc_t r, int voff, int soff) { return __builtin_amdgcn_raw_buffer_load_b128(r, voff, soff, 2); }
#define SB() __builtin_amdgcn_sched_barrier(0)
#define PIN4(a) asm volatile("" : "+v"(a[0]), "+v"(a[1]), "+v"(a[2]), "+v"(a[3])::"memory"); SB()
#define LGKM0() asm volatile("s_waitcnt lgkmcnt(0)" ::: "memory")
__device__ __forceinline__ f32x4 mfma(u32x4 a, u32x4 b, f32x4 c) {
  return __builtin_amdgcn_mfma_f32_16x16x32_bf16(__builtin_bit_cast(bf16x8, a), __builtin_bit_cast(bf16x8, b), c, 0, 0, 0);
}
__device__ __forceinline__ u32x4 bfrag(const u32x4* w, int e) {
  u32x4 o;
#pragma unroll
  for (int jj = 0; jj < 4; ++jj) o[jj] = __builtin_amdgcn_perm(w[2 * jj + 1][e >> 1], w[2 * jj][e >> 1], (e & 1) ? 0x07060302u : 0x05040100u);
  return o;
}
__device__ __forceinline__ u32x4 layernorm8(u32x4 xa) {
  float e[8];
#pragma unroll
  for (int k = 0; k < 4; ++k) { e[2 * k] = blo(xa[k]); e[2 * k + 1] = bhi(xa[k]); }
  const float mean = wave_sum(((e[0] + e[1]) + (e[2] + e[3])) + ((e[4] + e[5]) + (e[6] + e[7]))) * (1.0f / DM);
  float q = 0.f;
#pragma unroll
  for (int k = 0; k < 8; ++k) { const float d = e[k] - mean; q = fmaf(d, d, q); }
  const float var = wave_sum(q) * (1.0f / DM);
  const float mb = rbf(mean), sd = rbf(sqrtf(rbf(rbf(var) + 1e-5f)));
  const float ri = 1.0f / sd;
  u32x4 o;
#pragma unroll
  for (int k = 0; k < 4; ++k) {
    const float d0 = rbf(e[2 * k] - mb), d1 = rbf(e[2 * k + 1] - mb);
    float q0 = d0 * ri, q1 = d1 * ri;
    q0 = fmaf(fmaf(-q0, sd, d0), ri, q0); q1 = fmaf(fmaf(-q1, sd, d1), ri, q1);
    o[k] = pack2(q0, q1);
  }
  return o;
}

#define STAMP(i)
#define STAMP2(i)
#define STAMP_DECL
#define STAMP_FLUSH

constexpr int L_QP = 0;
constexpr int L_A = L_QP + 4 * DH;
constexpr int L_ML = L_A + 1024;
constexpr int L_CNT = L_ML + 16;
constexpr int L_XN = L_CNT + 16;
constexpr int L_GU = L_XN + 4 * 128;
constexpr int L_H = L_GU + 4 * 256;
constexpr int L_TOT = L_H + 4 * 64;

__global__ void __launch_bounds__(NTHR) attn_kernel(const u16* __restrict__ x, const u16* __restrict__ kc, const u16* __restrict__ vc,
                                                    const u16* __restrict__ wq, const u16* __restrict__ wo, const u16* __restrict__ w1,
                                                    const u16* __restrict__ w3, const u16* __restrict__ w2, float* __restrict__ ws) {
  __shared__ __attribute__((aligned(16))) unsigned lds[L_TOT];
  STAMP_DECL; STAMP(0);
  const int bid = blockIdx.x;
  const int tid = threadIdx.x, lane = tid & 63, wv = __builtin_amdgcn_readfirstlane(tid >> 6);
  const int j = bid >> 3, h = (bid & 7) * 4 + (j >> 3), b = (j >> 2) & 1, s = j & 3;
  const int r = lane >> 4, c = lane & 15;
  const bool fgrp = !ABL_FFN && (bid & 1) == 0 && (j >> 2) < 7;

  if (wv < 4) {
    const int a = wv;
    __builtin_amdgcn_s_setprio(3);
    const rsrc_t rx = mkrsrc(x, NB * DM * 2);
    const u32x4 xa = bld(rx, lane * 16, b * (DM * 2));
    u32x4 wr[32];
    const rsrc_t rq = mkrsrc(wq + h * DH, ABL_WQ ? 0 : (DM * NH * DH - h * DH) * 2);
    const int vq = r * (8 * NH * DH * 2) + c * 16;
#define LD_WQ(ks) { const int so_ = (128 * a + 32 * (((ks) + s) & 3)) * (NH * DH * 2); \
      _Pragma("unroll") for (int jj = 0; jj < 8; ++jj) wr[8 * (ks) + jj] = bld(rq, vq, so_ + jj * (NH * DH * 2)); } SB();
    LD_WQ(0)
    STAMP(1);
    STAMP(2);
    if (tid == 0) lds[L_CNT] = 0u;
    unsigned* xn = lds + L_XN + a * 128;
    {
      const u32x4 n0 = layernorm8(xa);
      if (r == a) *(u32x4*)(xn + 64 * b + 4 * c) = n0;
    }
    SB();
    STAMP(7);
    LD_WQ(1) LD_WQ(2) LD_WQ(3)
    STAMP(5);
    f32x4 d[8];
#pragma unroll
    for (int e = 0; e < 8; ++e) d[e] = f32x4{0.f, 0.f, 0.f, 0.f};
#pragma unroll
    for (int ks = 0; ks < 4; ++ks) {
      const u32x4 af = *(const u32x4*)(xn + 64 * b + 16 * ((ks + s) & 3) + 4 * r);
#pragma unroll
      for (int e = 0; e < 8; ++e) d[e] = mfma(af, bfrag(wr + 8 * ks, e), d[e]);
      SB();
    }
    if (r == 0) {
      *(f32x4*)(lds + L_QP + a * DH + 8 * c) = f32x4{d[0][0], d[1][0], d[2][0], d[3][0]};
      *(f32x4*)(lds + L_QP + a * DH + 8 * c + 4) = f32x4{d[4][0], d[5][0], d[6][0], d[7][0]};
    }
    LGKM0();
    STAMP(6);
    __builtin_amdgcn_s_barrier();
    if (fgrp && s == 0) {
      const int sl = ((bid & 7) >> 1) * 7 + (j >> 2), f0 = sl * FSL;
      const u32x4 xo = bld(rx, lane * 16, (b ^ 1) * (DM * 2));
      const GAS u32x4* p13 = (const GAS u32x4*)((c < 8 ? w1 : w3) + (size_t)(128 * a + 8 * r) * FF + f0 + 8 * (c & 7));
#pragma unroll
      for (int i = 0; i < 32; ++i) wr[i] = __builtin_nontemporal_load(p13 + (32 * (i >> 3) + (i & 7)) * (FF / 8));
      SB();
      {
        const u32x4 n1 = layernorm8(xo);
        if (r == a) *(u32x4*)(xn + 64 * (b ^ 1) + 4 * c) = n1;
      }
#pragma unroll
      for (int e = 0; e < 8; ++e) d[e] = f32x4{0.f, 0.f, 0.f, 0.f};
      const rsrc_t r2 = mkrsrc(w2 + (size_t)f0 * DM, FSL * DM * 2);
#pragma unroll
      for (int ks = 0; ks < 4; ++ks) {
        const u32x4 af = *(const u32x4*)(xn + 64 * (c & 1) + 16 * ks + 4 * r);
#pragma unroll
        for (int e = 0; e < 8; ++e) d[e] = mfma(af, bfrag(wr + 8 * ks, e), d[e]);
        SB();
        if (ks < 2) {
#pragma unroll
          for (int jj = 0; jj < 8; ++jj) wr[8 * ks + jj] = bld(r2, r * 8192 + c * 16 + (jj & 3) * 1024, ks * 32768 + (jj >> 2) * 4096 + a * 256);
          SB();
        }
      }
      if (r < 2) {
        float* gu = (float*)lds + L_GU + a * 256 + r * 128 + 8 * c;
        *(f32x4*)(gu) = r ? f32x4{d[0][1], d[1][1], d[2][1], d[3][1]} : f32x4{d[0][0], d[1][0], d[2][0], d[3][0]};
        *(f32x4*)(gu + 4) = r ? f32x4{d[4][1], d[5][1], d[6][1], d[7][1]} : f32x4{d[4][0], d[5][0], d[6][0], d[7][0]};
      }
      LGKM0();
      if (lane == 0) __hip_atomic_fetch_add((LAS unsigned*)(lds + L_CNT), 1u, __ATOMIC_RELAXED, __HIP_MEMORY_SCOPE_WORKGROUP);
      while (*(volatile LAS unsigned*)(lds + L_CNT) < 4u) __builtin_amdgcn_s_sleep(1);
      asm volatile("" ::: "memory");
      {
        const float* gu = (const float*)lds + L_GU + (lane >> 5) * 128 + 2 * (lane & 31);
        f32x2 g = {0.f, 0.f}, u = {0.f, 0.f};
#pragma unroll
        for (int aa = 0; aa < 4; ++aa) { g += *(const f32x2*)(gu + aa * 256); u += *(const f32x2*)(gu + aa * 256 + 64); }
        float hv[2];
#pragma unroll
        for (int k = 0; k < 2; ++k) { const float gg = rbf(g[k]); hv[k] = rbf(gg / (1.0f + __expf(-gg))) * rbf(u[k]); }
        lds[L_H + a * 64 + lane] = pack2(hv[0], hv[1]);
      }
#pragma unroll
      for (int e = 0; e < 8; ++e) d[e] = f32x4{0.f, 0.f, 0.f, 0.f};
#pragma unroll
      for (int ks = 0; ks < 2; ++ks) {
        const u32x4 af = *(const u32x4*)(lds + L_H + a * 64 + 32 * (c & 1) + 16 * ks + 4 * r);
#pragma unroll
        for (int e = 0; e < 8; ++e) d[e] = mfma(af, bfrag(wr + 8 * ks, e), d[e]);
      }
      if (r < 2) {
        float* fo = ws + WS_F + (size_t)(sl * 2 + r) * DM + 128 * a + 8 * c;
        *(f32x4*)(fo) = r ? f32x4{d[0][1], d[1][1], d[2][1], d[3][1]} : f32x4{d[0][0], d[1][0], d[2][0], d[3][0]};
        *(f32x4*)(fo + 4) = r ? f32x4{d[4][1], d[5][1], d[6][1], d[7][1]} : f32x4{d[4][0], d[5][0], d[6][0], d[7][0]};
      }
      SB();
    }
    u32x4 bf[32];
    {
      const rsrc_t ro = mkrsrc(wo + (size_t)h * DH * DM, ABL_WO ? 0 : DH * DM * 2);
#pragma unroll
      for (int ks = 0; ks < 4; ++ks) {
        __builtin_amdgcn_s_sleep(WO_SLEEP);
#pragma unroll
        for (int i = 0; i < 8; ++i) wr[i] = bld(ro, r * 8192 + c * 16 + (i & 3) * 1024, ks * 32768 + (i >> 2) * 4096 + a * 256);
        SB();
#pragma unroll
        for (int e = 0; e < 8; ++e) { bf[8 * ks + e] = bfrag(wr, e); asm volatile("" : "+v"(bf[8 * ks + e])); }
        SB();
      }
    }
    STAMP(4);
    __builtin_amdgcn_s_barrier();
    u32x4 af[4];
#pragma unroll
    for (int ks = 0; ks < 4; ++ks) af[ks] = *(const u32x4*)(lds + L_A + (ks * 64 + r * 16 + (c & 7)) * 4);
    const f32x4 ml0 = *(const f32x4*)(lds + L_ML), ml1 = *(const f32x4*)(lds + L_ML + 4);
#pragma unroll
    for (int e = 0; e < 8; ++e) d[e] = f32x4{0.f, 0.f, 0.f, 0.f};
#pragma unroll
    for (int ks = 0; ks < 4; ++ks)
#pragma unroll
      for (int e = 0; e < 8; ++e) d[e] = mfma(af[ks], bf[8 * ks + e], d[e]);
    const float M = fmaxf(fmaxf(ml0[0], ml0[2]), fmaxf(ml1[0], ml1[2]));
    const float e0 = __builtin_amdgcn_exp2f(ml0[0] - M), e1 = __builtin_amdgcn_exp2f(ml0[2] - M);
    const float e2 = __builtin_amdgcn_exp2f(ml1[0] - M), e3 = __builtin_amdgcn_exp2f(ml1[2] - M);
    const float L = (e0 * ml0[1] + e1 * ml0[3]) + (e2 * ml1[1] + e3 * ml1[3]);
    const float f0 = (r & 1) ? e2 : e0, f1 = (r & 1) ? e3 : e1;
    float v[4];
#pragma unroll
    for (int e = 0; e < 4; ++e) {
      const float p0 = d[e][0] + d[e][1], p1 = d[e][2] + d[e][3], q0 = d[4 + e][0] + d[4 + e][1], q1 = d[4 + e][2] + d[4 + e][3];
      v[e] = f0 * (r < 2 ? p0 : q0) + f1 * (r < 2 ? p1 : q1);
    }
    const f32x2 y = {t16_sum(v[0], v[2]), t16_sum(v[1], v[3])};
    const int slot = (b * NH + h) * NSPLIT + s;
    *(f32x2*)(ws + WS_Y + (size_t)slot * DM + 128 * a + 8 * c + 2 * r) = y;
    if (tid == 0) *(f32x2*)(ws + WS_ML + 2 * slot) = f32x2{M, L};
    STAMP(3); STAMP_FLUSH;
    return;
  }

  const int w = wv - 4;
  u32x4 ring[2 * RSL];
  const int vo = lane * 16;
  const int nst = fgrp ? (s == 0 ? NSTEP - 3 * FSH : NSTEP + FSH) : NSTEP;
  const int st0 = fgrp ? (s == 0 ? 0 : s * (NSTEP + FSH) - 4 * FSH) : s * NSTEP;
  const int lim = nst * STEPB;
  const size_t kvbase = ((size_t)(b * NH + h) * SK + (size_t)st0 * STEPK) * DH;
  const rsrc_t rk = mkrsrc(kc + kvbase, lim), rv = mkrsrc(vc + kvbase, lim);
  int so = w * (GL * 1024) + (((2 * s + b) * nst) >> 3) * STEPB;
  const int npad = (-nst) & (RG - 1);
#define ADV() so += STEPB; so = so >= lim ? so - lim : so;
#define FILL_K(g, o) _Pragma("unroll") for (int i = 0; i < GL; ++i) ring[(g) * GL + i] = bld(rk, vo + i * 1024, o); SB();
#define FILL_V(g, o) _Pragma("unroll") for (int i = 0; i < GL; ++i) ring[RSL + (g) * GL + i] = bld(rv, vo + i * 1024, o); SB();
#pragma unroll
  for (int g = 0; g < RG; ++g) {
    const int o = g < npad ? lim : so;
    FILL_K(g, o) FILL_V(g, o)
    if (g >= npad) { ADV() }
  }
  __builtin_amdgcn_s_barrier();
  unsigned qp[4];
  {
    f32x4 t0 = *(const f32x4*)(lds + L_QP + 8 * c), t1 = *(const f32x4*)(lds + L_QP + 8 * c + 4);
#pragma unroll
    for (int aa = 1; aa < 4; ++aa) {
      t0 += *(const f32x4*)(lds + L_QP + aa * DH + 8 * c);
      t1 += *(const f32x4*)(lds + L_QP + aa * DH + 8 * c + 4);
    }
    qp[0] = pack2(t0[0], t0[1]); qp[1] = pack2(t0[2], t0[3]); qp[2] = pack2(t1[0], t1[1]); qp[3] = pack2(t1[2], t1[3]);
  }
  STAMP(1);
  constexpr float SCALE = 0.088388347648318440f, LOG2E = 1.4426950408889634f;
  float m = -INFINITY, l = 0.f;
  f32x2 acc[4];
#pragma unroll
  for (int k = 0; k < 4; ++k) acc[k] = f32x2{0.f, 0.f};
  float sc[GL];

#define QK_PART(g)                                                                                   \
    _Pragma("unroll") for (int i = 0; i < GL; ++i) {                                                 \
      float d_ = 0.f;                                                                                \
      _Pragma("unroll") for (int k = 0; k < 4; ++k) d_ = dot2(ring[(g) * GL + i][k], qp[k], d_);     \
      sc[i] = d_;                                                                                    \
    }                                                                                                \
    PIN4(sc);
#define PV_PART(g)                                                                                   \
  {                                                                                                  \
    float mn = m;                                                                                    \
    _Pragma("unroll") for (int i = 0; i < GL; ++i) {                                                 \
      float t = row16_sum(sc[i]);                                                                    \
      if (MIMIC_SCORE_ROUND) t = rbf(rbf(t) * SCALE) * LOG2E; else t *= SCALE * LOG2E;               \
      sc[i] = t; mn = fmaxf(mn, t);                                                                  \
    }                                                                                                \
    const float al = __builtin_amdgcn_exp2f(m - mn);                                                 \
    m = mn; l *= al;                                                                                 \
    _Pragma("unroll") for (int k = 0; k < 4; ++k) acc[k] *= al;                                      \
    _Pragma("unroll") for (int i = 0; i < GL; ++i) {                                                 \
      const float p = __builtin_amdgcn_exp2f(sc[i] - mn);                                            \
      l += p;                                                                                        \
      const u32x4 vv = ring[RSL + (g) * GL + i];                                                     \
      _Pragma("unroll") for (int k = 0; k < 4; ++k)                                                  \
        acc[k] = __builtin_elementwise_fma(f32x2{p, p}, f32x2{blo(vv[k]), bhi(vv[k])}, acc[k]);      \
    }                                                                                                \
    PIN4(acc);                                                                                       \
  }
  int skip = npad;
  const int nrev = (nst + npad) / RG - 1;
#pragma nounroll
  for (int rev = 0; rev < nrev; ++rev) {
#define STEP(g) QK_PART(g) FILL_K(g, so) if ((g) == RG - 1 || skip <= (g)) PV_PART(g) FILL_V(g, so) ADV()
    STEP(0) STEP(1)
    STEP(2) STEP(3)
#undef STEP
    skip = 0;
  }
  STAMP(3);
  QK_PART(0) PV_PART(0) QK_PART(1) PV_PART(1)
  QK_PART(2) PV_PART(2) QK_PART(3) PV_PART(3)
  STAMP(2);
  {
    const float M = xrow_max(m);
    const float f = __builtin_amdgcn_exp2f(m - M);
    l = xrow_sum(l * f);
    float v[8];
#pragma unroll
    for (int k = 0; k < 4; ++k) { v[2 * k] = acc[k][0] * f; v[2 * k + 1] = acc[k][1] * f; }
    const f32x2 t = xrow_tsum8(v);
    const unsigned hi = pack2(t[0], t[1]), lo = pack2(t[0] - blo(hi), t[1] - bhi(hi));
    unsigned* pa = lds + L_A + ((c >> 2) * 64 + (c & 3) * 16 + 2 * w) * 4 + r;
    pa[0] = hi; pa[4] = lo;
    if (lane == 0) *(f32x2*)(lds + L_ML + 2 * w) = f32x2{M, l};
  }
  LGKM0();
  STAMP(4);
  __builtin_amdgcn_s_barrier();
  STAMP_FLUSH;
}

__global__ void __launch_bounds__(256) combine_kernel(const u16* __restrict__ x, const float* __restrict__ ws, u16* __restrict__ out) {
  __shared__ __attribute__((aligned(16))) float red[2][4][16];
  STAMP2(0);
  const int b = blockIdx.x >> 5, cg = blockIdx.x & 31;
  const int tid = threadIdx.x, lane = tid & 63, w = tid >> 6;
  const int q4 = tid & 3, pr = tid >> 2;
  const int col = cg * 16 + q4 * 4;
  const int p0 = b * 128 + pr, p1 = p0 + 64;
  const f32x4 y0 = *(const f32x4*)(ws + WS_Y + (size_t)p0 * DM + col);
  const f32x4 y1 = *(const f32x4*)(ws + WS_Y + (size_t)p1 * DM + col);
  const f32x4 ma0 = *(const f32x4*)(ws + WS_ML + 2 * (p0 & ~3)), ma1 = *(const f32x4*)(ws + WS_ML + 2 * (p0 & ~3) + 4);
  const f32x4 mb0 = *(const f32x4*)(ws + WS_ML + 2 * (p1 & ~3)), mb1 = *(const f32x4*)(ws + WS_ML + 2 * (p1 & ~3) + 4);
  f32x4 fv = {0.f, 0.f, 0.f, 0.f};
  if (pr < NFW) fv = *(const f32x4*)(ws + WS_F + (size_t)(pr * 2 + b) * DM + col);
  auto coef = [&](f32x4 a, f32x4 c, int sidx) {
    const float M = fmaxf(fmaxf(a[0], a[2]), fmaxf(c[0], c[2]));
    const float e0 = __builtin_amdgcn_exp2f(a[0] - M), e1 = __builtin_amdgcn_exp2f(a[2] - M);
    const float e2 = __builtin_amdgcn_exp2f(c[0] - M), e3 = __builtin_amdgcn_exp2f(c[2] - M);
    const float L = e0 * a[1] + e1 * a[3] + e2 * c[1] + e3 * c[3];
    const float e = sidx == 0 ? e0 : sidx == 1 ? e1 : sidx == 2 ? e2 : e3;
    return e / L;
  };
  const float c0 = coef(ma0, ma1, pr & 3), c1 = coef(mb0, mb1, pr & 3);
  float a[4], f[4];
#pragma unroll
  for (int k = 0; k < 4; ++k) {
    float t = c0 * y0[k] + c1 * y1[k], u = fv[k];
    t += dpp<ROR4>(t); u += dpp<ROR4>(u);
    t += dpp<ROR8>(t); u += dpp<ROR8>(u);
    a[k] = xrow_sum(t); f[k] = xrow_sum(u);
  }
  if (lane < 4) {
    *(f32x4*)(&red[0][w][lane * 4]) = f32x4{a[0], a[1], a[2], a[3]};
    *(f32x4*)(&red[1][w][lane * 4]) = f32x4{f[0], f[1], f[2], f[3]};
  }
  __syncthreads();
  if (tid < 16) {
    const float A = (red[0][0][tid] + red[0][1][tid]) + (red[0][2][tid] + red[0][3][tid]);
    const float F = (red[1][0][tid] + red[1][1][tid]) + (red[1][2][tid] + red[1][3][tid]);
    const int n = b * DM + cg * 16 + tid;
    const float xv = __builtin_bit_cast(float, (unsigned)x[n] << 16);
    const float r = rbf(xv + rbf(A)) + rbf(F);
    out[n] = __builtin_bit_cast(u16, (__bf16)r);
  }
  STAMP2(1);
}

}

extern "C" void kernel_launch(void* const* d_in, const int* in_sizes, int n_in, void* d_out, int out_size, void* d_ws,
                              size_t ws_size, hipStream_t stream) {
  const u16* x = (const u16*)d_in[0];
  float* ws = (float*)d_ws;
  attn_kernel<<<NWG, NTHR, 0, stream>>>(x, (const u16*)d_in[1], (const u16*)d_in[2], (const u16*)d_in[3], (const u16*)d_in[4],
                                        (const u16*)d_in[6], (const u16*)d_in[7], (const u16*)d_in[8], ws);
  combine_kernel<<<64, 256, 0, stream>>>(x, ws, (u16*)d_out);
}
```

```cpp
#include <hip/hip_runtime.h>
#include <hip/hip_bf16.h>
#include <cstdint>

#define ABL_WQ 0
#define ABL_WO 0
#define ABL_FFN 0
#define MIMIC_SCORE_ROUND 1
#define WQ_INFL 8
#define NEV 4
#define WO_SLEEP 32
#define CONS_PAR 0
#define POLL_MANY 16
#define POLL_SLEEP 16
#define CODE_PF 0
#define PF_MASK 7
#define WO_BATCH 1
#define XAUX 0
#define QAUX 0
#define OAUX 0
#define FSH 2

namespace {

constexpr int NB = 2, DM = 512, NH = 32, SK = 16384, DH = 128, FF = 1792;
constexpr int NSPLIT = 4;
constexpr int NSW = 4, NTHR = 512;
constexpr int NWG = NB * NH * NSPLIT;
constexpr int GL = 4;
#define RGV 2
constexpr int RG = RGV;
constexpr int NE = NEV;
constexpr int RSL = GL * (RG + NE);
constexpr int STEPK = NSW * GL * 4, STEPB = STEPK * DH * 2;
constexpr int NSTEP = SK / NSPLIT / STEPK;
constexpr int FSL = 64, NFW = FF / FSL;

constexpr int WS_E = 0;
constexpr int WS_ML = 4096;
constexpr int WS_F = WS_ML + NWG * 16;
constexpr int WS_Y = WS_F + NB * 32 * NFW * 128;
constexpr int WS_END = WS_Y + NB * 32 * 128 * 128;
constexpr int NFC = ABL_FFN ? 0 : NFW;

typedef unsigned u32x4 __attribute__((ext_vector_type(4)));
typedef float f32x4 __attribute__((ext_vector_type(4)));
typedef float f32x2 __attribute__((ext_vector_type(2)));
typedef __bf16 bf16x2 __attribute__((ext_vector_type(2)));
typedef __bf16 bf16x8 __attribute__((ext_vector_type(8)));
typedef unsigned short u16;
#define GAS __attribute__((address_space(1)))
#define LAS __attribute__((address_space(3)))

__device__ __forceinline__ float dot2(unsigned a, unsigned b, float acc) {
  return __builtin_amdgcn_fdot2_f32_bf16(__builtin_bit_cast(bf16x2, a), __builtin_bit_cast(bf16x2, b), acc, false);
}
__device__ __forceinline__ float blo(unsigned u) { return __builtin_bit_cast(float, u << 16); }
__device__ __forceinline__ float bhi(unsigned u) { return __builtin_bit_cast(float, u & 0xffff0000u); }
__device__ __forceinline__ float rbf(float f) { return (float)(__bf16)f; }
__device__ __forceinline__ unsigned pack2(float lo, float hi) {
  bf16x2 v; v.x = (__bf16)lo; v.y = (__bf16)hi; return __builtin_bit_cast(unsigned, v);
}
template <int CTRL> __device__ __forceinline__ float dpp(float x) {
  return __builtin_bit_cast(float, __builtin_amdgcn_mov_dpp(__builtin_bit_cast(int, x), CTRL, 0xf, 0xf, true));
}
constexpr int XOR1 = 0xB1, XOR2 = 0x4E, HMIR = 0x141, ROR8 = 0x128, ROR4 = 0x124;
__device__ __forceinline__ float row16_sum(float x) {
  x += dpp<XOR1>(x); x += dpp<XOR2>(x); x += dpp<HMIR>(x); x += dpp<ROR8>(x); return x;
}
__device__ __forceinline__ float t16_sum(float a, float b) {
  auto s = __builtin_amdgcn_permlane16_swap(__float_as_uint(a), __float_as_uint(b), false, false);
  return __uint_as_float(s[0]) + __uint_as_float(s[1]);
}
__device__ __forceinline__ float t32_sum(float a, float b) {
  auto s = __builtin_amdgcn_permlane32_swap(__float_as_uint(a), __float_as_uint(b), false, false);
  return __uint_as_float(s[0]) + __uint_as_float(s[1]);
}
__device__ __forceinline__ float xrow_sum(float x) { x = t16_sum(x, x); return t32_sum(x, x); }
__device__ __forceinline__ float xrow_max(float x) {
  auto s = __builtin_amdgcn_permlane16_swap(__float_as_uint(x), __float_as_uint(x), false, false);
  x = fmaxf(__uint_as_float(s[0]), __uint_as_float(s[1]));
  auto t = __builtin_amdgcn_permlane32_swap(__float_as_uint(x), __float_as_uint(x), false, false);
  return fmaxf(__uint_as_float(t[0]), __uint_as_float(t[1]));
}
__device__ __forceinline__ float wave_sum(float x) { return xrow_sum(row16_sum(x)); }
__device__ __forceinline__ f32x2 xrow_tsum8(const float (&v)[8]) {
  const float a0 = t32_sum(v[0], v[4]), a1 = t32_sum(v[1], v[5]), a2 = t32_sum(v[2], v[6]), a3 = t32_sum(v[3], v[7]);
  return f32x2{t16_sum(a0, a2), t16_sum(a1, a3)};
}
typedef __amdgpu_buffer_rsrc_t rsrc_t;
__device__ __forceinline__ rsrc_t mkrsrc(const void* p, int bytes) { return __builtin_amdgcn_make_buffer_rsrc((void*)p, 0, bytes, 0x00020000); }
__device__ __forceinline__ u32x4 bld(rsrc_t r, int voff, int soff) { return __builtin_amdgcn_raw_buffer_load_b128(r, voff, soff, 2); }
__device__ __forceinline__ u32x4 gld(rsrc_t r, int voff, int soff) { return __builtin_amdgcn_raw_buffer_load_b128(r, voff, soff, 16); }
__device__ __forceinline__ void gst(rsrc_t r, int voff, int soff, float v0, float v1, unsigned tag) {
  __builtin_amdgcn_raw_buffer_store_b128(u32x4{__float_as_uint(v0), tag, __float_as_uint(v1), tag}, r, voff, soff, 16);
}
#define SB() __builtin_amdgcn_sched_barrier(0)
__device__ __forceinline__ unsigned load_tag(const unsigned* ws) {
  unsigned e;
  asm volatile("s_load_dword %0, %1, 0x0\n\ts_waitcnt lgkmcnt(0)" : "=s"(e) : "s"(ws) : "memory");
  return e + 1u;
}
#define PIN4(a) asm volatile("" : "+v"(a[0]), "+v"(a[1]), "+v"(a[2]), "+v"(a[3])::"memory"); SB()
#define LGKM0() asm volatile("s_waitcnt lgkmcnt(0)" ::: "memory")
__device__ __forceinline__ f32x4 mfma(u32x4 a, u32x4 b, f32x4 c) {
  return __builtin_amdgcn_mfma_f32_16x16x32_bf16(__builtin_bit_cast(bf16x8, a), __builtin_bit_cast(bf16x8, b), c, 0, 0, 0);
}
__device__ __forceinline__ u32x4 bfrag(const u32x4* w, int e) {
  u32x4 o;
#pragma unroll
  for (int jj = 0; jj < 4; ++jj) o[jj] = __builtin_amdgcn_perm(w[2 * jj + 1][e >> 1], w[2 * jj][e >> 1], (e & 1) ? 0x07060302u : 0x05040100u);
  return o;
}
__device__ __forceinline__ u32x4 layernorm8(u32x4 xa) {
  float e[8];
#pragma unroll
  for (int k = 0; k < 4; ++k) { e[2 * k] = blo(xa[k]); e[2 * k + 1] = bhi(xa[k]); }
  const float mean = wave_sum(((e[0] + e[1]) + (e[2] + e[3])) + ((e[4] + e[5]) + (e[6] + e[7]))) * (1.0f / DM);
  float q = 0.f;
#pragma unroll
  for (int k = 0; k < 8; ++k) { const float d = e[k] - mean; q = fmaf(d, d, q); }
  const float var = wave_sum(q) * (1.0f / DM);
  const float mb = rbf(mean), sd = rbf(sqrtf(rbf(rbf(var) + 1e-5f)));
  const float ri = 1.0f / sd;
  u32x4 o;
#pragma unroll
  for (int k = 0; k < 4; ++k) {
    const float d0 = rbf(e[2 * k] - mb), d1 = rbf(e[2 * k + 1] - mb);
    float q0 = d0 * ri, q1 = d1 * ri;
    q0 = fmaf(fmaf(-q0, sd, d0), ri, q0); q1 = fmaf(fmaf(-q1, sd, d1), ri, q1);
    o[k] = pack2(q0, q1);
  }
  return o;
}

#define STAMP(i)
#define STAMP2(i)
#define STAMP_DECL
#define STAMP_FLUSH

constexpr int L_QP = 0;
constexpr int L_A = L_QP + 4 * DH;
constexpr int L_ML = L_A + 1024;
constexpr int L_CNT = L_ML + 16, L_TAG = L_CNT + 1;
constexpr int L_RED = L_CNT + 16;
constexpr int L_XN = L_RED + 128;
constexpr int L_GU = L_XN + 4 * 128;
constexpr int L_H = L_GU + 4 * 256;
constexpr int L_TOT = L_H + 4 * 64;

__global__ void __launch_bounds__(NTHR, 2) attn_kernel(const u16* __restrict__ kc, const u16* __restrict__ vc, const u16* __restrict__ x,
                                                       const u16* __restrict__ wq, unsigned* ws, const u16* __restrict__ wo,
                                                       const u16* __restrict__ w1, const u16* __restrict__ w3, const u16* __restrict__ w2,
                                                       u16* __restrict__ out) {
  __shared__ __attribute__((aligned(16))) unsigned lds[L_TOT];
  STAMP_DECL; STAMP(0);
  const int bid = blockIdx.x;
  const int tid = threadIdx.x, lane = tid & 63, wv = __builtin_amdgcn_readfirstlane(tid >> 6);
  const int j = bid >> 3, h = (bid & 7) * 4 + (j >> 3), b = (j >> 2) & 1, s = j & 3;
  const int r = lane >> 4, c = lane & 15;
  const bool fgrp = !ABL_FFN && (bid & 1) == 0 && (j >> 2) < 7;

  if (wv < 4) {
    const int a = wv;
    __builtin_amdgcn_s_setprio(3);
    const rsrc_t rx = mkrsrc(x, NB * DM * 2);
    const u32x4 xa = __builtin_amdgcn_raw_buffer_load_b128(rx, lane * 16, b * (DM * 2), XAUX);
    u32x4 wr[32];
    const rsrc_t rq = mkrsrc(wq + h * DH, ABL_WQ ? 0 : (DM * NH * DH - h * DH) * 2);
    const int vq = r * (8 * NH * DH * 2) + c * 16;
#define LD_WQ(i) { if ((i) >= WQ_INFL) asm volatile("s_waitcnt vmcnt(%0)" ::"n"(WQ_INFL - 1) : "memory"); \
      wr[i] = __builtin_amdgcn_raw_buffer_load_b128(rq, vq, (128 * a + 32 * ((((i) >> 3) + s) & 3) + ((i) & 7)) * (NH * DH * 2), QAUX); SB(); }
#pragma unroll
    for (int i = 0; i < WQ_INFL; ++i) LD_WQ(i)
    STAMP(1);
    if (tid == 0) lds[L_CNT] = 0u;
    unsigned* xn = lds + L_XN + a * 128;
    {
      const u32x4 n0 = layernorm8(xa);
      if (r == a) *(u32x4*)(xn + 64 * b + 4 * c) = n0;
    }
    SB();
    STAMP(7);
    f32x4 d[8];
#pragma unroll
    for (int e = 0; e < 8; ++e) d[e] = f32x4{0.f, 0.f, 0.f, 0.f};
#pragma unroll
    for (int ks = 0; ks < 4; ++ks) {
#pragma unroll
      for (int i = 8 * ks + WQ_INFL; i < 8 * ks + 8 + WQ_INFL; ++i) if (i < 32) LD_WQ(i)
      const u32x4 af = *(const u32x4*)(xn + 64 * b + 16 * ((ks + s) & 3) + 4 * r);
#pragma unroll
      for (int e = 0; e < 8; ++e) d[e] = mfma(af, bfrag(wr + 8 * ks, e), d[e]);
      SB();
    }
    if (r == 0) {
      *(f32x4*)(lds + L_QP + a * DH + 8 * c) = f32x4{d[0][0], d[1][0], d[2][0], d[3][0]};
      *(f32x4*)(lds + L_QP + a * DH + 8 * c + 4) = f32x4{d[4][0], d[5][0], d[6][0], d[7][0]};
    }
    LGKM0();
    STAMP(6);
    __builtin_amdgcn_s_barrier();
    const unsigned tag = load_tag(ws);
    if (tid == 0) lds[L_TAG] = tag;
    const rsrc_t rw = mkrsrc(ws, WS_END);
    u32x4 bf[32];
    {
      const rsrc_t ro = mkrsrc(wo + (size_t)h * DH * DM, ABL_WO ? 0 : DH * DM * 2);
#pragma unroll
      for (int ks = 0; ks < 4; ++ks) {
#pragma unroll
        for (int i = 0; i < 8; ++i) {
          if (i % WO_BATCH == 0) __builtin_amdgcn_s_sleep(WO_SLEEP);
          wr[i] = __builtin_amdgcn_raw_buffer_load_b128(ro, r * 8192 + c * 16 + (i & 3) * 1024, ks * 32768 + (i >> 2) * 4096 + a * 256, OAUX);
          SB();
        }
#pragma unroll
        for (int e = 0; e < 8; ++e) { bf[8 * ks + e] = bfrag(wr, e); asm volatile("" : "+v"(bf[8 * ks + e])); }
        SB();
      }
    }
    if (fgrp && s == 0) {
      const int sl = ((bid & 7) >> 1) * 7 + (j >> 2), f0 = sl * FSL;
      const u32x4 xo = __builtin_amdgcn_raw_buffer_load_b128(rx, lane * 16, (b ^ 1) * (DM * 2), XAUX);
      const GAS u32x4* p13 = (const GAS u32x4*)((c < 8 ? w1 : w3) + (size_t)(128 * a + 8 * r) * FF + f0 + 8 * (c & 7));
#pragma unroll
      for (int i = 0; i < 8; ++i) wr[i] = __builtin_nontemporal_load(p13 + i * (FF / 8));
      SB();
      {
        const u32x4 n1 = layernorm8(xo);
        if (r == a) *(u32x4*)(xn + 64 * (b ^ 1) + 4 * c) = n1;
      }
#pragma unroll
      for (int e = 0; e < 8; ++e) d[e] = f32x4{0.f, 0.f, 0.f, 0.f};
      const rsrc_t r2 = mkrsrc(w2 + (size_t)f0 * DM, FSL * DM * 2);
#pragma unroll
      for (int ks = 0; ks < 4; ++ks) {
        const u32x4 af = *(const u32x4*)(xn + 64 * (c & 1) + 16 * ks + 4 * r);
#pragma unroll
        for (int e = 0; e < 8; ++e) d[e] = mfma(af, bfrag(wr, e), d[e]);
        SB();
#pragma unroll
        for (int jj = 0; jj < 8; ++jj)
          wr[jj] = ks < 3 ? __builtin_nontemporal_load(p13 + (32 * (ks + 1) + jj) * (FF / 8))
                          : bld(r2, r * 8192 + c * 16 + (jj & 3) * 1024, (jj >> 2) * 4096 + a * 256);
        SB();
      }
      if (r < 2) {
        float* gu = (float*)lds + L_GU + a * 256 + r * 128 + 8 * c;
        *(f32x4*)(gu) = r ? f32x4{d[0][1], d[1][1], d[2][1], d[3][1]} : f32x4{d[0][0], d[1][0], d[2][0], d[3][0]};
        *(f32x4*)(gu + 4) = r ? f32x4{d[4][1], d[5][1], d[6][1], d[7][1]} : f32x4{d[4][0], d[5][0], d[6][0], d[7][0]};
      }
      LGKM0();
      if (lane == 0) __hip_atomic_fetch_add((LAS unsigned*)(lds + L_CNT), 1u, __ATOMIC_RELAXED, __HIP_MEMORY_SCOPE_WORKGROUP);
      while (*(volatile LAS unsigned*)(lds + L_CNT) < 4u) __builtin_amdgcn_s_sleep(1);
      asm volatile("" ::: "memory");
      {
        const float* gu = (const float*)lds + L_GU + (lane >> 5) * 128 + 2 * (lane & 31);
        f32x2 g = {0.f, 0.f}, u = {0.f, 0.f};
#pragma unroll
        for (int aa = 0; aa < 4; ++aa) { g += *(const f32x2*)(gu + aa * 256); u += *(const f32x2*)(gu + aa * 256 + 64); }
        float hv[2];
#pragma unroll
        for (int k = 0; k < 2; ++k) { const float gg = rbf(g[k]); hv[k] = rbf(gg / (1.0f + __expf(-gg))) * rbf(u[k]); }
        lds[L_H + a * 64 + lane] = pack2(hv[0], hv[1]);
      }
#pragma unroll
      for (int e = 0; e < 8; ++e) d[e] = f32x4{0.f, 0.f, 0.f, 0.f};
#pragma unroll
      for (int ks = 0; ks < 2; ++ks) {
        const u32x4 af = *(const u32x4*)(lds + L_H + a * 64 + 32 * (c & 1) + 16 * ks + 4 * r);
#pragma unroll
        for (int e = 0; e < 8; ++e) d[e] = mfma(af, bfrag(wr, e), d[e]);
        SB();
        if (ks == 0) {
#pragma unroll
          for (int jj = 0; jj < 8; ++jj) wr[jj] = bld(r2, r * 8192 + c * 16 + (jj & 3) * 1024, 32768 + (jj >> 2) * 4096 + a * 256);
          SB();
        }
      }
      {
        const int fo = WS_F + (((r & 1) * 32 + 8 * a + (c >> 1)) * NFW + sl) * 128 + (c & 1) * 64 + (r >> 1) * 32;
        float t[4];
#pragma unroll
        for (int e = 0; e < 4; ++e) {
          const float p0 = (r & 1) ? d[e][1] : d[e][0], p1 = (r & 1) ? d[4 + e][1] : d[4 + e][0];
          t[e] = r < 2 ? p0 : p1;
        }
        gst(rw, fo, 0, t[0], t[1], tag); gst(rw, fo + 16, 0, t[2], t[3], tag);
      }
      SB();
    }
    LGKM0();
    STAMP(4);
    __builtin_amdgcn_s_barrier();
    u32x4 af[4];
#pragma unroll
    for (int ks = 0; ks < 4; ++ks) af[ks] = *(const u32x4*)(lds + L_A + (ks * 64 + r * 16 + (c & 7)) * 4);
    const f32x4 ml0 = *(const f32x4*)(lds + L_ML), ml1 = *(const f32x4*)(lds + L_ML + 4);
#pragma unroll
    for (int e = 0; e < 8; ++e) d[e] = f32x4{0.f, 0.f, 0.f, 0.f};
#pragma unroll
    for (int ks = 0; ks < 4; ++ks)
#pragma unroll
      for (int e = 0; e < 8; ++e) d[e] = mfma(af[ks], bf[8 * ks + e], d[e]);
    const float M = fmaxf(fmaxf(ml0[0], ml0[2]), fmaxf(ml1[0], ml1[2]));
    const float e0 = __builtin_amdgcn_exp2f(ml0[0] - M), e1 = __builtin_amdgcn_exp2f(ml0[2] - M);
    const float e2 = __builtin_amdgcn_exp2f(ml1[0] - M), e3 = __builtin_amdgcn_exp2f(ml1[2] - M);
    const float L = (e0 * ml0[1] + e1 * ml0[3]) + (e2 * ml1[1] + e3 * ml1[3]);
    const float f0 = (r & 1) ? e2 : e0, f1 = (r & 1) ? e3 : e1;
    float v[4];
#pragma unroll
    for (int e = 0; e < 4; ++e) {
      const float p0 = d[e][0] + d[e][1], p1 = d[e][2] + d[e][3], q0 = d[4 + e][0] + d[4 + e][1], q1 = d[4 + e][2] + d[4 + e][3];
      v[e] = f0 * (r < 2 ? p0 : q0) + f1 * (r < 2 ? p1 : q1);
    }
    const f32x2 y = {t16_sum(v[0], v[2]), t16_sum(v[1], v[3])};
    const int pidx = h * NSPLIT + s;
    gst(rw, (c >> 1) * (128 * 128) + (c & 1) * 64 + r * 16, WS_Y + ((b * 32 + 8 * a) * 128 + pidx) * 128, y[0], y[1], tag);
    if (tid == 0) gst(rw, 0, WS_ML + (b * 128 + pidx) * 16, M, L, tag);
    STAMP(3); STAMP_FLUSH;
    return;
  }

  const int w = wv - 4;
  u32x4 ring[2 * RSL];
  const int vo = lane * 16;
  const int nst = fgrp ? (s == 0 ? NSTEP - 3 * FSH : NSTEP + FSH) : NSTEP;
  const int st0 = fgrp ? (s == 0 ? 0 : s * (NSTEP + FSH) - 4 * FSH) : s * NSTEP;
  const int lim = nst * STEPB;
  const size_t kvbase = ((size_t)(b * NH + h) * SK + (size_t)st0 * STEPK) * DH;
  const rsrc_t rk = mkrsrc(kc + kvbase, lim), rv = mkrsrc(vc + kvbase, lim);
  int so = w * (GL * 1024) + (((2 * s + b) * nst) >> 3) * STEPB;
#define ADV() so += STEPB; so = so >= lim ? so - lim : so;
#define FILL_K(g, o) _Pragma("unroll") for (int i = 0; i < GL; ++i) { ring[(g) * GL + i] = bld(rk, vo + i * 1024, o); SB(); }
#define FILL_V(g, o) _Pragma("unroll") for (int i = 0; i < GL; ++i) { ring[RSL + (g) * GL + i] = bld(rv, vo + i * 1024, o); SB(); }
#pragma unroll
  for (int g = RG; g < RG + NE; ++g) { FILL_K(g, so) if (g == RG) STAMP(5); FILL_V(g, so) ADV() }
#pragma unroll
  for (int g = 0; g < RG; ++g) { FILL_K(g, so) FILL_V(g, so) ADV() }
  STAMP(6);
  __builtin_amdgcn_s_barrier();
  unsigned qp[4];
  {
    f32x4 t0 = *(const f32x4*)(lds + L_QP + 8 * c), t1 = *(const f32x4*)(lds + L_QP + 8 * c + 4);
#pragma unroll
    for (int aa = 1; aa < 4; ++aa) {
      t0 += *(const f32x4*)(lds + L_QP + aa * DH + 8 * c);
      t1 += *(const f32x4*)(lds + L_QP + aa * DH + 8 * c + 4);
    }
    qp[0] = pack2(t0[0], t0[1]); qp[1] = pack2(t0[2], t0[3]); qp[2] = pack2(t1[0], t1[1]); qp[3] = pack2(t1[2], t1[3]);
  }
  STAMP(1);
  constexpr float SCALE = 0.088388347648318440f, LOG2E = 1.4426950408889634f;
  float m = -INFINITY, l = 0.f;
  f32x2 acc[4];
#pragma unroll
  for (int k = 0; k < 4; ++k) acc[k] = f32x2{0.f, 0.f};
  float sc[GL];

#define QK_PART(g)                                                                                   \
    _Pragma("unroll") for (int i = 0; i < GL; ++i) {                                                 \
      float d_ = 0.f;                                                                                \
      _Pragma("unroll") for (int k = 0; k < 4; ++k) d_ = dot2(ring[(g) * GL + i][k], qp[k], d_);     \
      sc[i] = d_;                                                                                    \
    }                                                                                                \
    PIN4(sc);
#define PV_PART(g)                                                                                   \
  {                                                                                                  \
                  \
    const float s0_ = sc[0] + dpp<XOR1>(sc[0]), s1_ = sc[1] + dpp<XOR1>(sc[1]);                      \
    const float s2_ = sc[2] + dpp<XOR1>(sc[2]), s3_ = sc[3] + dpp<XOR1>(sc[3]);                      \
    const float u0 = (c & 1) ? s1_ : s0_, u1 = (c & 1) ? s3_ : s2_;                                  \
    const float w0_ = u0 + dpp<XOR2>(u0), w1_ = u1 + dpp<XOR2>(u1);                                  \
    float t = (c & 2) ? w1_ : w0_;                                                                   \
    t += dpp<ROR4>(t); t += dpp<ROR8>(t);                                                            \
    if (MIMIC_SCORE_ROUND) t = rbf(rbf(t) * SCALE) * LOG2E; else t *= SCALE * LOG2E;                 \
    float mn = fmaxf(t, dpp<XOR1>(t)); mn = fmaxf(fmaxf(mn, dpp<XOR2>(mn)), m);                      \
    const float al = __builtin_amdgcn_exp2f(m - mn), p = __builtin_amdgcn_exp2f(t - mn);             \
    m = mn; l = fmaf(l, al, p);                                                                      \
    _Pragma("unroll") for (int k = 0; k < 4; ++k) acc[k] *= al;                                      \
    _Pragma("unroll") for (int i = 0; i < GL; ++i) {                                                 \
      const float pb = i == 0 ? dpp<0x00>(p) : i == 1 ? dpp<0x55>(p) : i == 2 ? dpp<0xAA>(p) : dpp<0xFF>(p); \
      const u32x4 vv = ring[RSL + (g) * GL + i];                                                     \
      _Pragma("unroll") for (int k = 0; k < 4; ++k)                                                  \
        acc[k] = __builtin_elementwise_fma(f32x2{pb, pb}, f32x2{blo(vv[k]), bhi(vv[k])}, acc[k]);    \
    }                                                                                                \
    PIN4(acc);                                                                                       \
  }
  int left = nst - RG - NE, ex = 0;
#pragma nounroll
  do {
#define STEP(g) if constexpr ((g) < RG) { const int o_ = left > (g) ? so : lim; QK_PART(g) FILL_K(g, o_) PV_PART(g) FILL_V(g, o_) ADV() }
    STEP(0) STEP(1) STEP(2) STEP(3) STEP(4) STEP(5)
#undef STEP
#define XSTEP(e) if constexpr ((e) < NE) { if (ex == (e)) { PIN4((ring + (RG + (e)) * GL)); PIN4((ring + RSL + (RG + (e)) * GL)); QK_PART(RG + (e)) PV_PART(RG + (e)) } }
    XSTEP(0) XSTEP(1) XSTEP(2) XSTEP(3) XSTEP(4)
#undef XSTEP
    ++ex;
    left -= RG;
  } while (left > 0);
  STAMP(3);
#define STEP(g) if constexpr ((g) < RG) { if ((g) == 0 || left + RG > (g)) { QK_PART(g) PV_PART(g) } }
  STEP(0) STEP(1) STEP(2) STEP(3) STEP(4) STEP(5)
#undef STEP
  STAMP(2);
  {
    const float M = xrow_max(m);
    const float f = __builtin_amdgcn_exp2f(m - M);
    l += dpp<XOR1>(l); l += dpp<XOR2>(l);
    l = xrow_sum(l * f);
    float v[8];
#pragma unroll
    for (int k = 0; k < 4; ++k) { v[2 * k] = acc[k][0] * f; v[2 * k + 1] = acc[k][1] * f; }
    const f32x2 t = xrow_tsum8(v);
    const unsigned hi = pack2(t[0], t[1]), lo = pack2(t[0] - blo(hi), t[1] - bhi(hi));
    unsigned* pa = lds + L_A + ((c >> 2) * 64 + (c & 3) * 16 + 2 * w) * 4 + r;
    pa[0] = hi; pa[4] = lo;
    if (lane == 0) *(f32x2*)(lds + L_ML + 2 * w) = f32x2{M, l};
  }
  LGKM0();
  STAMP(4);
  __builtin_amdgcn_s_barrier();
  if ((bid & 1) == CONS_PAR && j < 16) {
    const int ci = ((bid & 7) >> 1) * 16 + j, cb = ci >> 5, cg = ci & 31;
    const int t = tid - 256, pc = t & 7, hh = t >> 3;
    const unsigned tag = lds[L_TAG];
    const rsrc_t rw = mkrsrc(ws, WS_END);
    const int sy = WS_Y + (cb * 32 + cg) * (128 * 128), sm = WS_ML + cb * 2048;
    int vy = hh * 512 + pc * 16, vm = hh * 64;
    int vf = hh < NFC ? WS_F + ((cb * 32 + cg) * NFW + hh) * 128 + pc * 16 : sm + vm;
    int vx = ci == 0 ? WS_ML + 2048 + (t & 127) * 16 : vf;
    unsigned xv = 0;
    if (t < 8) xv = *(const unsigned*)(x + cb * DM + cg * 16 + 2 * t);
    float k0 = 0.f, k1 = 0.f, g0 = 0.f, g1 = 0.f, a0;
    bool done = false;
    for (unsigned spin = 0; spin < (1u << 20); ++spin) {
      asm volatile("" ::: "memory");
      u32x4 yy[4], mm[4];
#pragma unroll
      for (int i = 0; i < 4; ++i) { mm[i] = gld(rw, vm + i * 16, sm); yy[i] = gld(rw, vy + i * 128, sy); }
      const u32x4 ff = gld(rw, vf, 0);
      const u32x4 oo = gld(rw, vx, 0);
      unsigned bad = (ff[1] ^ tag) | (ff[3] ^ tag) | (oo[1] ^ tag);
#pragma unroll
      for (int i = 0; i < 4; ++i) bad |= (yy[i][1] ^ tag) | (yy[i][3] ^ tag) | (mm[i][1] ^ tag) | (mm[i][3] ^ tag);
      const float M = fmaxf(fmaxf(__uint_as_float(mm[0][0]), __uint_as_float(mm[1][0])), fmaxf(__uint_as_float(mm[2][0]), __uint_as_float(mm[3][0])));
      float L = 0.f, n0 = 0.f, n1 = 0.f;
#pragma unroll
      for (int i = 0; i < 4; ++i) {
        const float e = __builtin_amdgcn_exp2f(__uint_as_float(mm[i][0]) - M);
        L = fmaf(e, __uint_as_float(mm[i][2]), L); n0 = fmaf(e, __uint_as_float(yy[i][0]), n0); n1 = fmaf(e, __uint_as_float(yy[i][2]), n1);
      }
      const float ri = 1.0f / L;
      const bool take = !done && bad == 0u;
      k0 = take ? n0 * ri : k0; k1 = take ? n1 * ri : k1;
      g0 = take && hh < NFC ? __uint_as_float(ff[0]) : g0; g1 = take && hh < NFC ? __uint_as_float(ff[2]) : g1;
      done = done || take;
      vy = done ? WS_END : vy; vm = done ? WS_END : vm; vf = done ? WS_END : vf; vx = done ? WS_END : vx;
      a0 = t32_sum(k0 + dpp<ROR8>(k0), g0 + dpp<ROR8>(g0));
      const float a1 = t32_sum(k1 + dpp<ROR8>(k1), g1 + dpp<ROR8>(g1));
      a0 = t16_sum(a0, a1);
      const unsigned long long miss = __builtin_amdgcn_ballot_w64(!done);
      if (miss == 0ull) break;
      if (__builtin_popcountll(miss) > POLL_MANY) __builtin_amdgcn_s_sleep(POLL_SLEEP);
    }
    if (c < 8) lds[L_RED + w * 32 + (r >> 1) * 16 + 2 * c + (r & 1)] = __float_as_uint(a0);
    LGKM0();
    if (lane == 0) __hip_atomic_fetch_add((LAS unsigned*)(lds + L_CNT), 16u, __ATOMIC_RELAXED, __HIP_MEMORY_SCOPE_WORKGROUP);
    if (w == 0) {
      while (*(volatile LAS unsigned*)(lds + L_CNT) < 64u) {}
      asm volatile("" ::: "memory");
      if (t < 8) {
        const float* red = (const float*)lds + L_RED + 2 * t;
        const f32x2 A = (*(const f32x2*)(red) + *(const f32x2*)(red + 32)) + (*(const f32x2*)(red + 64) + *(const f32x2*)(red + 96));
        const f32x2 F = (*(const f32x2*)(red + 16) + *(const f32x2*)(red + 48)) + (*(const f32x2*)(red + 80) + *(const f32x2*)(red + 112));
        *(unsigned*)(out + cb * DM + cg * 16 + 2 * t) = pack2(rbf(blo(xv) + rbf(A[0])) + rbf(F[0]), rbf(bhi(xv) + rbf(A[1])) + rbf(F[1]));
      }
      if (ci == 0 && t == 0) __builtin_amdgcn_raw_buffer_store_b32(tag, rw, WS_E, 0, 16);
    }
  }
  STAMP(7);
  STAMP_FLUSH;
}

}

extern "C" void kernel_launch(void* const* d_in, const int* in_sizes, int n_in, void* d_out, int out_size, void* d_ws,
                              size_t ws_size, hipStream_t stream) {
  attn_kernel<<<NWG, NTHR, 0, stream>>>((const u16*)d_in[1], (const u16*)d_in[2], (const u16*)d_in[0], (const u16*)d_in[3], (unsigned*)d_ws,
                                        (const u16*)d_in[4], (const u16*)d_in[6], (const u16*)d_in[7], (const u16*)d_in[8], (u16*)d_out);
}
```

```cpp
#include <hip/hip_runtime.h>
#include <hip/hip_bf16.h>
#include <cstdint>

#define WQ_INFL 8
#define WO_SLEEP 32
#define CONS_PAR 0
#define POLL_MANY 16
#define POLL_SLEEP 16
#define OAUX 2
#define FSH 2
#define NXR 2

namespace {

constexpr int NB = 2, DM = 512, NH = 32, SK = 16384, DH = 128, FF = 1792;
constexpr int NSPLIT = 4;
constexpr int NTHR = 512;
constexpr int NWG = NB * NH * NSPLIT;
constexpr int STEPK = 64, STEPB = STEPK * DH * 2;
constexpr int NSTEP = SK / NSPLIT / STEPK;
constexpr int NREVMAX = (NSTEP + FSH + 3) / 4;
constexpr int FSL = 64, NFW = FF / FSL;

constexpr int WS_E = 0;
constexpr int WS_ML = 4096;
constexpr int WS_F = WS_ML + NB * NH * 256;
constexpr int WS_Y = WS_F + NB * 32 * NFW * 128;
constexpr int WS_END = WS_Y + NB * 32 * 128 * 128;
constexpr unsigned SPIN_CAP = 1u << 20;

typedef unsigned u32x4 __attribute__((ext_vector_type(4)));
typedef float f32x4 __attribute__((ext_vector_type(4)));
typedef float f32x2 __attribute__((ext_vector_type(2)));
typedef __bf16 bf16x2 __attribute__((ext_vector_type(2)));
typedef __bf16 bf16x8 __attribute__((ext_vector_type(8)));
typedef unsigned short u16;
#define GAS __attribute__((address_space(1)))
#define LAS __attribute__((address_space(3)))

__device__ __forceinline__ float dot2(unsigned a, unsigned b, float acc) {
  return __builtin_amdgcn_fdot2_f32_bf16(__builtin_bit_cast(bf16x2, a), __builtin_bit_cast(bf16x2, b), acc, false);
}
__device__ __forceinline__ float blo(unsigned u) { return __builtin_bit_cast(float, u << 16); }
__device__ __forceinline__ float bhi(unsigned u) { return __builtin_bit_cast(float, u & 0xffff0000u); }
__device__ __forceinline__ float rbf(float f) { return (float)(__bf16)f; }
__device__ __forceinline__ unsigned pack2(float lo, float hi) {
  bf16x2 v; v.x = (__bf16)lo; v.y = (__bf16)hi; return __builtin_bit_cast(unsigned, v);
}
template <int CTRL> __device__ __forceinline__ float dpp(float x) {
  return __builtin_bit_cast(float, __builtin_amdgcn_mov_dpp(__builtin_bit_cast(int, x), CTRL, 0xf, 0xf, true));
}
constexpr int XOR1 = 0xB1, XOR2 = 0x4E, XOR7 = 0x141, XOR8 = 0x128, BCAST = 0x150;
__device__ __forceinline__ float row16_sum(float x) {
  x += dpp<XOR1>(x); x += dpp<XOR2>(x); x += dpp<XOR7>(x); x += dpp<XOR8>(x); return x;
}
__device__ __forceinline__ float row16_max(float x) {
  x = fmaxf(x, dpp<XOR1>(x)); x = fmaxf(x, dpp<XOR2>(x)); x = fmaxf(x, dpp<XOR7>(x)); return fmaxf(x, dpp<XOR8>(x));
}
__device__ __forceinline__ float t16_sum(float a, float b) {
  auto s = __builtin_amdgcn_permlane16_swap(__float_as_uint(a), __float_as_uint(b), false, false);
  return __uint_as_float(s[0]) + __uint_as_float(s[1]);
}
__device__ __forceinline__ float t32_sum(float a, float b) {
  auto s = __builtin_amdgcn_permlane32_swap(__float_as_uint(a), __float_as_uint(b), false, false);
  return __uint_as_float(s[0]) + __uint_as_float(s[1]);
}
__device__ __forceinline__ float xrow_sum(float x) { x = t16_sum(x, x); return t32_sum(x, x); }
__device__ __forceinline__ float xrow_max(float x) {
  auto s = __builtin_amdgcn_permlane16_swap(__float_as_uint(x), __float_as_uint(x), false, false);
  x = fmaxf(__uint_as_float(s[0]), __uint_as_float(s[1]));
  auto t = __builtin_amdgcn_permlane32_swap(__float_as_uint(x), __float_as_uint(x), false, false);
  return fmaxf(__uint_as_float(t[0]), __uint_as_float(t[1]));
}
__device__ __forceinline__ float wave_sum(float x) { return xrow_sum(row16_sum(x)); }
typedef __amdgpu_buffer_rsrc_t rsrc_t;
__device__ __forceinline__ rsrc_t mkrsrc(const void* p, int bytes) { return __builtin_amdgcn_make_buffer_rsrc((void*)p, 0, bytes, 0x00020000); }
__device__ __forceinline__ u32x4 bld(rsrc_t r, int voff, int soff) { return __builtin_amdgcn_raw_buffer_load_b128(r, voff, soff, 2); }
__device__ __forceinline__ u32x4 bldc(rsrc_t r, int voff, int soff) { return __builtin_amdgcn_raw_buffer_load_b128(r, voff, soff, 0); }
__device__ __forceinline__ u32x4 gld(rsrc_t r, int voff, int soff) { return __builtin_amdgcn_raw_buffer_load_b128(r, voff, soff, 16); }
__device__ __forceinline__ void gst(rsrc_t r, int voff, int soff, float v0, float v1, unsigned tag) {
  __builtin_amdgcn_raw_buffer_store_b128(u32x4{__float_as_uint(v0), tag, __float_as_uint(v1), tag}, r, voff, soff, 16);
}
#define SB() __builtin_amdgcn_sched_barrier(0)
__device__ __forceinline__ unsigned load_tag(const unsigned* ws) {
  unsigned e;
  asm volatile("s_load_dword %0, %1, 0x0\n\ts_waitcnt lgkmcnt(0)" : "=s"(e) : "s"(ws) : "memory");
  return e + 1u;
}
#define PIN4(a) asm volatile("" : "+v"((a)[0]), "+v"((a)[1]), "+v"((a)[2]), "+v"((a)[3])::"memory"); SB()
#define LGKM0() asm volatile("s_waitcnt lgkmcnt(0)" ::: "memory")
__device__ __forceinline__ f32x4 mfma(u32x4 a, u32x4 b, f32x4 c) {
  return __builtin_amdgcn_mfma_f32_16x16x32_bf16(__builtin_bit_cast(bf16x8, a), __builtin_bit_cast(bf16x8, b), c, 0, 0, 0);
}
__device__ __forceinline__ u32x4 bfrag(const u32x4* w, int e) {
  u32x4 o;
#pragma unroll
  for (int jj = 0; jj < 4; ++jj) o[jj] = __builtin_amdgcn_perm(w[2 * jj + 1][e >> 1], w[2 * jj][e >> 1], (e & 1) ? 0x07060302u : 0x05040100u);
  return o;
}
__device__ __forceinline__ u32x4 layernorm8(u32x4 xa) {
  float e[8];
#pragma unroll
  for (int k = 0; k < 4; ++k) { e[2 * k] = blo(xa[k]); e[2 * k + 1] = bhi(xa[k]); }
  const float mean = wave_sum(((e[0] + e[1]) + (e[2] + e[3])) + ((e[4] + e[5]) + (e[6] + e[7]))) * (1.0f / DM);
  float q = 0.f;
#pragma unroll
  for (int k = 0; k < 8; ++k) { const float d = e[k] - mean; q = fmaf(d, d, q); }
  const float var = wave_sum(q) * (1.0f / DM);
  const float mb = rbf(mean), sd = rbf(sqrtf(rbf(rbf(var) + 1e-5f)));
  const float ri = 1.0f / sd;
  u32x4 o;
#pragma unroll
  for (int k = 0; k < 4; ++k) {
    const float d0 = rbf(e[2 * k] - mb), d1 = rbf(e[2 * k + 1] - mb);
    float q0 = d0 * ri, q1 = d1 * ri;
    q0 = fmaf(fmaf(-q0, sd, d0), ri, q0); q1 = fmaf(fmaf(-q1, sd, d1), ri, q1);
    o[k] = pack2(q0, q1);
  }
  return o;
}

#define STAMP(i)
#define STAMP_DECL
#define STAMP_FLUSH

constexpr int L_QP = 0;
constexpr int L_A = L_QP + 4 * DH;
constexpr int L_CNT = L_A + 1024, L_TAG = L_CNT + 1;
constexpr int L_RED = L_CNT + 16;
constexpr int L_XN = L_RED + 128;
constexpr int L_GU = L_XN + 4 * 128;
constexpr int L_H = L_GU + 4 * 256;
constexpr int L_S = L_H + 4 * 64;
constexpr int L_TOT = L_S + 4 * NREVMAX * 64;

__global__ void __launch_bounds__(NTHR, 2) attn_kernel(const u16* __restrict__ kc, const u16* __restrict__ vc, const u16* __restrict__ x,
                                                       const u16* __restrict__ wq, unsigned* ws, u16* __restrict__ out,
                                                       const u16* __restrict__ wo, const u16* __restrict__ w2,
                                                       const u16* __restrict__ w1, const u16* __restrict__ w3) {
  __shared__ __attribute__((aligned(16))) unsigned lds[L_TOT];
  STAMP_DECL; STAMP(0);
  const int bid = blockIdx.x;
  const int tid = threadIdx.x, lane = tid & 63, wv = __builtin_amdgcn_readfirstlane(tid >> 6);
  const int j = bid >> 3, h = (bid & 7) * 4 + (j >> 3), b = (j >> 2) & 1, s = j & 3;
  const int r = lane >> 4, c = lane & 15;
  const bool fgrp = (bid & 1) == 0 && (j >> 2) < 7;
  const int w = wv - 4;
  const int fs = fgrp ? FSH : 0;
  int nst = NSTEP + (s == 0 ? -3 * fs : fs);
  int kvb = ((b * NH + h) * NSPLIT * NSTEP + s * NSTEP + (s == 0 ? 0 : (s - 4) * fs)) * STEPB;
  int so0 = w * 4096 + (((2 * s + b) * nst) >> 3) * STEPB;
  asm volatile("" : "+s"(nst), "+s"(kvb), "+s"(so0) : "s"(kc), "s"(vc), "s"(x), "s"(wq), "s"(ws), "s"(out));

  if (wv < 4) {
    const int a = wv;
    __builtin_amdgcn_s_setprio(3);
    const rsrc_t rx = mkrsrc(x, NB * DM * 2);
    const u32x4 xa = bldc(rx, lane * 16, b * (DM * 2));
    u32x4 wr[32];
    const rsrc_t rq = mkrsrc(wq + h * DH, (DM * NH * DH - h * DH) * 2);
    const int vq = r * (8 * NH * DH * 2) + c * 16;
#define LD_WQ(i) { if ((i) >= WQ_INFL) asm volatile("s_waitcnt vmcnt(%0)" ::"n"(WQ_INFL - 1) : "memory"); \
      wr[i] = bldc(rq, vq, (128 * a + 32 * ((((i) >> 3) + s) & 3) + ((i) & 7)) * (NH * DH * 2)); SB(); }
#pragma unroll
    for (int i = 0; i < WQ_INFL; ++i) LD_WQ(i)
    STAMP(1);
    if (a == 0) {
      const unsigned t = load_tag(ws);
      if (lane == 0) { lds[L_CNT] = 0u; lds[L_TAG] = t; }
    }
    SB();
    unsigned* xn = lds + L_XN + a * 128;
    {
      const u32x4 n0 = layernorm8(xa);
      if (r == a) *(u32x4*)(xn + 64 * b + 4 * c) = n0;
    }
    SB();
    STAMP(7);
    f32x4 d[8];
#pragma unroll
    for (int e = 0; e < 8; ++e) d[e] = f32x4{0.f, 0.f, 0.f, 0.f};
#pragma unroll
    for (int ks = 0; ks < 4; ++ks) {
#pragma unroll
      for (int i = 8 * ks + WQ_INFL; i < 8 * ks + 8 + WQ_INFL; ++i) if (i < 32) LD_WQ(i)
      const u32x4 af = *(const u32x4*)(xn + 64 * b + 16 * ((ks + s) & 3) + 4 * r);
#pragma unroll
      for (int e = 0; e < 8; ++e) d[e] = mfma(af, bfrag(wr + 8 * ks, e), d[e]);
      SB();
    }
#undef LD_WQ
    if (r == 0) {
      *(f32x4*)(lds + L_QP + a * DH + 8 * c) = f32x4{d[0][0], d[1][0], d[2][0], d[3][0]};
      *(f32x4*)(lds + L_QP + a * DH + 8 * c + 4) = f32x4{d[4][0], d[5][0], d[6][0], d[7][0]};
    }
    LGKM0();
    STAMP(6);
    __builtin_amdgcn_s_barrier();
    const unsigned tag = lds[L_TAG];
    const rsrc_t rw = mkrsrc(ws, WS_END);
    u32x4 bf[32];
    {
      const rsrc_t ro = mkrsrc(wo + (size_t)h * DH * DM, DH * DM * 2);
#pragma unroll
      for (int ks = 0; ks < 4; ++ks) {
#pragma unroll
        for (int i = 0; i < 8; ++i) {
          __builtin_amdgcn_s_sleep(WO_SLEEP);
          wr[i] = __builtin_amdgcn_raw_buffer_load_b128(ro, r * 8192 + c * 16 + (i & 3) * 1024, ks * 32768 + (i >> 2) * 4096 + a * 256, OAUX);
          SB();
        }
#pragma unroll
        for (int e = 0; e < 8; ++e) { bf[8 * ks + e] = bfrag(wr, e); asm volatile("" : "+v"(bf[8 * ks + e])); }
        SB();
      }
    }
    if (fgrp && s == 0) {
      const int sl = ((bid & 7) >> 1) * 7 + (j >> 2), f0 = sl * FSL;
      const u32x4 xo = bldc(rx, lane * 16, (b ^ 1) * (DM * 2));
      const GAS u32x4* p13 = (const GAS u32x4*)((c < 8 ? w1 : w3) + (size_t)(128 * a + 8 * r) * FF + f0 + 8 * (c & 7));
#pragma unroll
      for (int i = 0; i < 8; ++i) wr[i] = __builtin_nontemporal_load(p13 + i * (FF / 8));
      SB();
      {
        const u32x4 n1 = layernorm8(xo);
        if (r == a) *(u32x4*)(xn + 64 * (b ^ 1) + 4 * c) = n1;
      }
#pragma unroll
      for (int e = 0; e < 8; ++e) d[e] = f32x4{0.f, 0.f, 0.f, 0.f};
      const rsrc_t r2 = mkrsrc(w2 + (size_t)f0 * DM, FSL * DM * 2);
#pragma unroll
      for (int ks = 0; ks < 4; ++ks) {
        const u32x4 af = *(const u32x4*)(xn + 64 * (c & 1) + 16 * ks + 4 * r);
#pragma unroll
        for (int e = 0; e < 8; ++e) d[e] = mfma(af, bfrag(wr, e), d[e]);
        SB();
#pragma unroll
        for (int jj = 0; jj < 8; ++jj)
          wr[jj] = ks < 3 ? __builtin_nontemporal_load(p13 + (32 * (ks + 1) + jj) * (FF / 8))
                          : bld(r2, r * 8192 + c * 16 + (jj & 3) * 1024, (jj >> 2) * 4096 + a * 256);
        SB();
      }
      if (r < 2) {
        float* gu = (float*)lds + L_GU + a * 256 + r * 128 + 8 * c;
        *(f32x4*)(gu) = r ? f32x4{d[0][1], d[1][1], d[2][1], d[3][1]} : f32x4{d[0][0], d[1][0], d[2][0], d[3][0]};
        *(f32x4*)(gu + 4) = r ? f32x4{d[4][1], d[5][1], d[6][1], d[7][1]} : f32x4{d[4][0], d[5][0], d[6][0], d[7][0]};
      }
      LGKM0();
      if (lane == 0) __hip_atomic_fetch_add((LAS unsigned*)(lds + L_CNT), 1u, __ATOMIC_RELAXED, __HIP_MEMORY_SCOPE_WORKGROUP);
      while (*(volatile LAS unsigned*)(lds + L_CNT) < 4u) __builtin_amdgcn_s_sleep(1);
      asm volatile("" ::: "memory");
      {
        const float* gu = (const float*)lds + L_GU + (lane >> 5) * 128 + 2 * (lane & 31);
        f32x2 g = {0.f, 0.f}, u = {0.f, 0.f};
#pragma unroll
        for (int aa = 0; aa < 4; ++aa) { g += *(const f32x2*)(gu + aa * 256); u += *(const f32x2*)(gu + aa * 256 + 64); }
        float hv[2];
#pragma unroll
        for (int k = 0; k < 2; ++k) { const float gg = rbf(g[k]); hv[k] = rbf(gg / (1.0f + __expf(-gg))) * rbf(u[k]); }
        lds[L_H + a * 64 + lane] = pack2(hv[0], hv[1]);
      }
#pragma unroll
      for (int e = 0; e < 8; ++e) d[e] = f32x4{0.f, 0.f, 0.f, 0.f};
#pragma unroll
      for (int ks = 0; ks < 2; ++ks) {
        const u32x4 af = *(const u32x4*)(lds + L_H + a * 64 + 32 * (c & 1) + 16 * ks + 4 * r);
#pragma unroll
        for (int e = 0; e < 8; ++e) d[e] = mfma(af, bfrag(wr, e), d[e]);
        SB();
        if (ks == 0) {
#pragma unroll
          for (int jj = 0; jj < 8; ++jj) wr[jj] = bld(r2, r * 8192 + c * 16 + (jj & 3) * 1024, 32768 + (jj >> 2) * 4096 + a * 256);
          SB();
        }
      }
      {
        const int fo = WS_F + (((r & 1) * 32 + 8 * a + (c >> 1)) * NFW + sl) * 128 + (c & 1) * 64 + (r >> 1) * 32;
        float t[4];
#pragma unroll
        for (int e = 0; e < 4; ++e) {
          const float p0 = (r & 1) ? d[e][1] : d[e][0], p1 = (r & 1) ? d[4 + e][1] : d[4 + e][0];
          t[e] = r < 2 ? p0 : p1;
        }
        gst(rw, fo, 0, t[0], t[1], tag); gst(rw, fo + 16, 0, t[2], t[3], tag);
      }
      SB();
    }
    float f0, f1;
    {
      u32x4 ml;
      for (unsigned spin = 0; spin < SPIN_CAP; ++spin) {
        asm volatile("" ::: "memory");
        ml = gld(rw, c * 16, WS_ML + (b * NH + h) * 256);
        if (__builtin_amdgcn_ballot_w64(((ml[1] ^ tag) | (ml[3] ^ tag)) != 0u) == 0ull) break;
        __builtin_amdgcn_s_sleep(32);
      }
      const float mw = __uint_as_float(ml[0]), M = row16_max(mw);
      const float e = __builtin_amdgcn_exp2f(mw - M);
      const float f = e / row16_sum(e * __uint_as_float(ml[2]));
      const int src = (4 * s + 2 * (r & 1)) * 4;
      f0 = __builtin_bit_cast(float, __builtin_amdgcn_ds_bpermute(src, __builtin_bit_cast(int, f)));
      f1 = __builtin_bit_cast(float, __builtin_amdgcn_ds_bpermute(src + 4, __builtin_bit_cast(int, f)));
      asm volatile("" : "+v"(f0), "+v"(f1)::"memory");
    }
    STAMP(4);
    __builtin_amdgcn_s_barrier();
    u32x4 af[4];
#pragma unroll
    for (int ks = 0; ks < 4; ++ks) af[ks] = *(const u32x4*)(lds + L_A + (ks * 64 + r * 16 + (c & 7)) * 4);
#pragma unroll
    for (int e = 0; e < 8; ++e) d[e] = f32x4{0.f, 0.f, 0.f, 0.f};
#pragma unroll
    for (int ks = 0; ks < 4; ++ks)
#pragma unroll
      for (int e = 0; e < 8; ++e) d[e] = mfma(af[ks], bf[8 * ks + e], d[e]);
    float v[4];
#pragma unroll
    for (int e = 0; e < 4; ++e) {
      const float p0 = d[e][0] + d[e][1], p1 = d[e][2] + d[e][3], q0 = d[4 + e][0] + d[4 + e][1], q1 = d[4 + e][2] + d[4 + e][3];
      v[e] = f0 * (r < 2 ? p0 : q0) + f1 * (r < 2 ? p1 : q1);
    }
    gst(rw, (c >> 1) * (128 * 128) + (c & 1) * 64 + r * 16, WS_Y + ((b * 32 + 8 * a) * 128 + h * NSPLIT + s) * 128,
        t16_sum(v[0], v[2]), t16_sum(v[1], v[3]), tag);
    STAMP(3); STAMP_FLUSH;
    return;
  }

  u32x4 ring[16 * (1 + NXR)];
  const int vo = lane * 16;
  const int lim = nst * STEPB;
  const rsrc_t rk = mkrsrc((const char*)kc + (unsigned)kvb, lim), rv = mkrsrc((const char*)vc + (unsigned)kvb, lim);
  int so = so0;
#define ADV() so += STEPB; so = so >= lim ? so - lim : so;
#define FILL(rs, g, o) _Pragma("unroll") for (int i = 0; i < 4; ++i) { ring[(g) * 4 + i] = bld(rs, vo + i * 1024, o); SB(); }
#pragma unroll
  for (int g = 4; g < 4 + 4 * NXR; ++g) { FILL(rk, g, so) if (g == 4) STAMP(5); ADV() }
#pragma unroll
  for (int g = 0; g < 4; ++g) { FILL(rk, g, so) ADV() }
  STAMP(6);
  __builtin_amdgcn_s_barrier();
  unsigned qp[4];
  {
    f32x4 t0 = *(const f32x4*)(lds + L_QP + 8 * c), t1 = *(const f32x4*)(lds + L_QP + 8 * c + 4);
#pragma unroll
    for (int aa = 1; aa < 4; ++aa) {
      t0 += *(const f32x4*)(lds + L_QP + aa * DH + 8 * c);
      t1 += *(const f32x4*)(lds + L_QP + aa * DH + 8 * c + 4);
    }
    qp[0] = pack2(t0[0], t0[1]); qp[1] = pack2(t0[2], t0[3]); qp[2] = pack2(t1[0], t1[1]); qp[3] = pack2(t1[2], t1[3]);
  }
  STAMP(1);
  constexpr float SCALE = 0.088388347648318440f, LOG2E = 1.4426950408889634f;
  float* sl = (float*)lds + L_S + w * (NREVMAX * 64) + lane;
  float m = -INFINITY;
  float xs[16];
#define QK(g, slot)                                                                                  \
    _Pragma("unroll") for (int i = 0; i < 4; ++i) {                                                  \
      float d_ = 0.f;                                                                                \
      _Pragma("unroll") for (int k = 0; k < 4; ++k) d_ = dot2(ring[(slot) * 4 + i][k], qp[k], d_);   \
      xs[(g) * 4 + i] = d_;                                                                          \
    }                                                                                                \
    PIN4(xs + (g) * 4);
#define TR(HALF, CTRL) _Pragma("unroll") for (int k = 0; k < (HALF); ++k) {                          \
      const float t1_ = xs[k] + dpp<CTRL>(xs[k]), t2_ = xs[k + (HALF)] + dpp<CTRL>(xs[k + (HALF)]);  \
      xs[k] = (c & (HALF)) ? t2_ : t1_; }
#define SCORE(rev, nvalid)                                                                           \
  {                                                                                                  \
    TR(8, XOR8) TR(4, XOR7) TR(2, XOR2) TR(1, XOR1)                                                  \
    float t = rbf(rbf(xs[0]) * SCALE) * LOG2E;                     \
    t = (c >> 2) < (nvalid) ? t : -INFINITY;                                                         \
    m = fmaxf(m, t);                                                                                 \
    sl[(rev) * 64] = t;                                                                              \
    SB();                                                                                            \
  }
  const int nrev = (nst + 3) >> 2;
  {
    int left = nst - 4 * (1 + NXR);
    rsrc_t rn = rk;
#pragma nounroll
    for (int rev = NXR; rev < nrev; ++rev) {
      if (rev == nrev - 1) { rn = rv; so = so0; left = 4; }
#define STEP(g) { const int o_ = left > (g) ? so : lim; QK(g, g) FILL(rn, g, o_) ADV() }
      STEP(0) STEP(1) STEP(2) STEP(3)
#undef STEP
      SCORE(rev, nst - 4 * rev)
      left -= 4;
#define XREV(e) if constexpr ((e) < NXR) { if (rev == NXR + (e)) {                                   \
        _Pragma("unroll") for (int g = 0; g < 4; ++g) { PIN4(ring + (4 * (1 + (e)) + g) * 4); QK(g, 4 * (1 + (e)) + g) } \
        SCORE(e, 4) } }
      XREV(0) XREV(1)
#undef XREV
    }
  }
  {
    m = xrow_max(row16_max(m));
    float l = 0.f;
#pragma unroll
    for (int rev = 0; rev < NREVMAX; ++rev) {
      const float t = rev < NREVMAX - 3 || rev < nrev ? sl[rev * 64] : -INFINITY;
      const float p = __builtin_amdgcn_exp2f(t - m);
      l += p;
      sl[rev * 64] = p;
    }
    l = wave_sum(l);
    gst(mkrsrc(ws, WS_END), lane == 0 ? 0 : WS_END, WS_ML + (b * NH + h) * 256 + (4 * s + w) * 16, m, l, lds[L_TAG]);
    SB();
  }
  STAMP(3);
  f32x2 acc[4];
#pragma unroll
  for (int k = 0; k < 4; ++k) acc[k] = f32x2{0.f, 0.f};
#define PV1(g, i) { const float pb = dpp<BCAST + 4 * (g) + (i)>(p); const u32x4 vv = ring[(g) * 4 + (i)];  \
      _Pragma("unroll") for (int k = 0; k < 4; ++k)                                                  \
        acc[k] = __builtin_elementwise_fma(f32x2{pb, pb}, f32x2{blo(vv[k]), bhi(vv[k])}, acc[k]); }
#define PV(g) PV1(g, 0) PV1(g, 1) PV1(g, 2) PV1(g, 3) PIN4(acc);
  int left = nst - 4;
  float pn = sl[0];
#pragma nounroll
  for (int rev = 1; rev < nrev; ++rev) {
    const float p = pn;
    pn = sl[rev * 64];
#define STEP(g) { const int o_ = left > (g) ? so : lim; PV(g) FILL(rv, g, o_) ADV() }
    STEP(0) STEP(1) STEP(2) STEP(3)
#undef STEP
    left -= 4;
  }
  {
    const float p = pn;
    PV(0)
    if (left > -3) { PV(1) }
    if (left > -2) { PV(2) }
    if (left > -1) { PV(3) }
  }
  STAMP(2);
  {
    const float a0 = t32_sum(acc[0][0], acc[2][0]), a1 = t32_sum(acc[0][1], acc[2][1]);
    const float a2 = t32_sum(acc[1][0], acc[3][0]), a3 = t32_sum(acc[1][1], acc[3][1]);
    const float t0 = t16_sum(a0, a2), t1 = t16_sum(a1, a3);
    const unsigned hi = pack2(t0, t1), lo = pack2(t0 - blo(hi), t1 - bhi(hi));
    unsigned* pa = lds + L_A + ((c >> 2) * 64 + (c & 3) * 16 + 2 * w) * 4 + r;
    pa[0] = hi; pa[4] = lo;
  }
  LGKM0();
  STAMP(4);
  __builtin_amdgcn_s_barrier();
  if ((bid & 1) == CONS_PAR && j < 16) {
    const int ci = ((bid & 7) >> 1) * 16 + j, cb = ci >> 5, cg = ci & 31;
    const int t = tid - 256, pc = t & 7, hh = t >> 3;
    const unsigned tag = lds[L_TAG];
    const rsrc_t rw = mkrsrc(ws, WS_END);
    const int sy = WS_Y + (cb * 32 + cg) * (128 * 128);
    int vy = hh * 512 + pc * 16;
    int vf = hh < NFW ? WS_F + ((cb * 32 + cg) * NFW + hh) * 128 + pc * 16 : sy + vy;
    int vx = ci == 0 ? WS_ML + NH * 256 + (t & 127) * 64 : vf;
    unsigned xv = 0;
    if (t < 8) xv = *(const unsigned*)(x + cb * DM + cg * 16 + 2 * t);
    float k0 = 0.f, k1 = 0.f, g0 = 0.f, g1 = 0.f, a0;
    bool done = false;
    for (unsigned spin = 0; spin < SPIN_CAP; ++spin) {
      asm volatile("" ::: "memory");
      u32x4 yy[4];
#pragma unroll
      for (int i = 0; i < 4; ++i) yy[i] = gld(rw, vy + i * 128, sy);
      const u32x4 ff = gld(rw, vf, 0);
      const u32x4 oo = gld(rw, vx, 0);
      unsigned bad = (ff[1] ^ tag) | (ff[3] ^ tag) | (oo[1] ^ tag);
#pragma unroll
      for (int i = 0; i < 4; ++i) bad |= (yy[i][1] ^ tag) | (yy[i][3] ^ tag);
      const bool take = !done && bad == 0u;
      k0 = take ? (__uint_as_float(yy[0][0]) + __uint_as_float(yy[1][0])) + (__uint_as_float(yy[2][0]) + __uint_as_float(yy[3][0])) : k0;
      k1 = take ? (__uint_as_float(yy[0][2]) + __uint_as_float(yy[1][2])) + (__uint_as_float(yy[2][2]) + __uint_as_float(yy[3][2])) : k1;
      g0 = take && hh < NFW ? __uint_as_float(ff[0]) : g0; g1 = take && hh < NFW ? __uint_as_float(ff[2]) : g1;
      done = done || take;
      vy = done ? WS_END : vy; vf = done ? WS_END : vf; vx = done ? WS_END : vx;
      a0 = t32_sum(k0 + dpp<XOR8>(k0), g0 + dpp<XOR8>(g0));
      const float a1 = t32_sum(k1 + dpp<XOR8>(k1), g1 + dpp<XOR8>(g1));
      a0 = t16_sum(a0, a1);
      const unsigned long long miss = __builtin_amdgcn_ballot_w64(!done);
      if (miss == 0ull) break;
      if (__builtin_popcountll(miss) > POLL_MANY) __builtin_amdgcn_s_sleep(POLL_SLEEP);
    }
    if (c < 8) lds[L_RED + w * 32 + (r >> 1) * 16 + 2 * c + (r & 1)] = __float_as_uint(a0);
    LGKM0();
    if (lane == 0) __hip_atomic_fetch_add((LAS unsigned*)(lds + L_CNT), 16u, __ATOMIC_RELAXED, __HIP_MEMORY_SCOPE_WORKGROUP);
    if (w == 0) {
      while (*(volatile LAS unsigned*)(lds + L_CNT) < 64u) {}
      asm volatile("" ::: "memory");
      if (t < 8) {
        const float* red = (const float*)lds + L_RED + 2 * t;
        const f32x2 A = (*(const f32x2*)(red) + *(const f32x2*)(red + 32)) + (*(const f32x2*)(red + 64) + *(const f32x2*)(red + 96));
        const f32x2 F = (*(const f32x2*)(red + 16) + *(const f32x2*)(red + 48)) + (*(const f32x2*)(red + 80) + *(const f32x2*)(red + 112));
        *(unsigned*)(out + cb * DM + cg * 16 + 2 * t) = pack2(rbf(blo(xv) + rbf(A[0])) + rbf(F[0]), rbf(bhi(xv) + rbf(A[1])) + rbf(F[1]));
      }
      if (ci == 0 && t == 0) __builtin_amdgcn_raw_buffer_store_b32(tag, rw, WS_E, 0, 16);
    }
  }
  STAMP(7);
  STAMP_FLUSH;
}

}

extern "C" void kernel_launch(void* const* d_in, const int* in_sizes, int n_in, void* d_out, int out_size, void* d_ws,
                              size_t ws_size, hipStream_t stream) {
  attn_kernel<<<NWG, NTHR, 0, stream>>>((const u16*)d_in[1], (const u16*)d_in[2], (const u16*)d_in[0], (const u16*)d_in[3], (unsigned*)d_ws,
                                        (u16*)d_out, (const u16*)d_in[4], (const u16*)d_in[8], (const u16*)d_in[6], (const u16*)d_in[7]);
}
```

```cpp
#include <hip/hip_runtime.h>
#include <hip/hip_bf16.h>
#include <cstdint>
#include <cstdio>

#define WQ_INFL 8
#define WO_SLEEP 32
#define CONS_PAR 0
#define POLL_MANY 32
#define POLL_SLEEP 16
#define OAUX 2
#define PHASE_FIX 1
#define ABL 0
#define ML_SLEEP 100
#define PIPE 0
#define FSH 2
#define NXR 2

namespace {

constexpr int NB = 2, DM = 512, NH = 32, SK = 16384, DH = 128, FF = 1792;
constexpr int NSPLIT = 4;
constexpr int NTHR = 512;
constexpr int NWG = NB * NH * NSPLIT;
constexpr int STEPK = 64, STEPB = STEPK * DH * 2;
constexpr int NSTEP = SK / NSPLIT / STEPK;
constexpr int NREVMAX = (NSTEP + FSH + 3) / 4;
constexpr int FSL = 64, NFW = FF / FSL;

constexpr int WS_E = 0;
constexpr int WS_ML = 4096;
constexpr int WS_F = WS_ML + NB * NH * 256;
constexpr int WS_Y = WS_F + NB * 32 * NFW * 128;
constexpr int WS_END = WS_Y + NB * 32 * 128 * 128;
constexpr unsigned SPIN_CAP = 1u << 20;

typedef unsigned u32x4 __attribute__((ext_vector_type(4)));
typedef float f32x4 __attribute__((ext_vector_type(4)));
typedef float f32x2 __attribute__((ext_vector_type(2)));
typedef __bf16 bf16x2 __attribute__((ext_vector_type(2)));
typedef __bf16 bf16x8 __attribute__((ext_vector_type(8)));
typedef unsigned short u16;
#define GAS __attribute__((address_space(1)))
#define LAS __attribute__((address_space(3)))

__device__ __forceinline__ float dot2(unsigned a, unsigned b, float acc) {
  return __builtin_amdgcn_fdot2_f32_bf16(__builtin_bit_cast(bf16x2, a), __builtin_bit_cast(bf16x2, b), acc, false);
}
__device__ __forceinline__ float blo(unsigned u) { return __builtin_bit_cast(float, u << 16); }
__device__ __forceinline__ float bhi(unsigned u) { return __builtin_bit_cast(float, u & 0xffff0000u); }
__device__ __forceinline__ float rbf(float f) { return (float)(__bf16)f; }
__device__ __forceinline__ unsigned pack2(float lo, float hi) {
  bf16x2 v; v.x = (__bf16)lo; v.y = (__bf16)hi; return __builtin_bit_cast(unsigned, v);
}
template <int CTRL> __device__ __forceinline__ float dpp(float x) {
  return __builtin_bit_cast(float, __builtin_amdgcn_mov_dpp(__builtin_bit_cast(int, x), CTRL, 0xf, 0xf, true));
}
constexpr int XOR1 = 0xB1, XOR2 = 0x4E, XOR7 = 0x141, XOR8 = 0x128, BCAST = 0x150;
__device__ __forceinline__ float row16_sum(float x) {
  x += dpp<XOR1>(x); x += dpp<XOR2>(x); x += dpp<XOR7>(x); x += dpp<XOR8>(x); return x;
}
__device__ __forceinline__ float row16_max(float x) {
  x = fmaxf(x, dpp<XOR1>(x)); x = fmaxf(x, dpp<XOR2>(x)); x = fmaxf(x, dpp<XOR7>(x)); return fmaxf(x, dpp<XOR8>(x));
}
__device__ __forceinline__ float t16_sum(float a, float b) {
  auto s = __builtin_amdgcn_permlane16_swap(__float_as_uint(a), __float_as_uint(b), false, false);
  return __uint_as_float(s[0]) + __uint_as_float(s[1]);
}
__device__ __forceinline__ float t32_sum(float a, float b) {
  auto s = __builtin_amdgcn_permlane32_swap(__float_as_uint(a), __float_as_uint(b), false, false);
  return __uint_as_float(s[0]) + __uint_as_float(s[1]);
}
__device__ __forceinline__ float xrow_sum(float x) { x = t16_sum(x, x); return t32_sum(x, x); }
__device__ __forceinline__ float xrow_max(float x) {
  auto s = __builtin_amdgcn_permlane16_swap(__float_as_uint(x), __float_as_uint(x), false, false);
  x = fmaxf(__uint_as_float(s[0]), __uint_as_float(s[1]));
  auto t = __builtin_amdgcn_permlane32_swap(__float_as_uint(x), __float_as_uint(x), false, false);
  return fmaxf(__uint_as_float(t[0]), __uint_as_float(t[1]));
}
__device__ __forceinline__ float wave_sum(float x) { return xrow_sum(row16_sum(x)); }
typedef __amdgpu_buffer_rsrc_t rsrc_t;
__device__ __forceinline__ rsrc_t mkrsrc(const void* p, int bytes) { return __builtin_amdgcn_make_buffer_rsrc((void*)p, 0, bytes, 0x00020000); }
__device__ __forceinline__ u32x4 bld(rsrc_t r, int voff, int soff) { return __builtin_amdgcn_raw_buffer_load_b128(r, voff, soff, 2); }
__device__ __forceinline__ u32x4 bldc(rsrc_t r, int voff, int soff) { return __builtin_amdgcn_raw_buffer_load_b128(r, voff, soff, 0); }
__device__ __forceinline__ u32x4 gld(rsrc_t r, int voff, int soff) { return __builtin_amdgcn_raw_buffer_load_b128(r, voff, soff, 16); }
__device__ __forceinline__ void gst(rsrc_t r, int voff, int soff, float v0, float v1, unsigned tag) {
  __builtin_amdgcn_raw_buffer_store_b128(u32x4{__float_as_uint(v0), tag, __float_as_uint(v1), tag}, r, voff, soff, 16);
}
#define SB() __builtin_amdgcn_sched_barrier(0)
__device__ __forceinline__ unsigned load_tag(const unsigned* ws) {
  unsigned e;
  asm volatile("s_load_dword %0, %1, 0x0\n\ts_waitcnt lgkmcnt(0)" : "=s"(e) : "s"(ws) : "memory");
  return e + 1u;
}
#define PIN4(a) asm volatile("" : "+v"((a)[0]), "+v"((a)[1]), "+v"((a)[2]), "+v"((a)[3])::"memory"); SB()
#define LGKM0() asm volatile("s_waitcnt lgkmcnt(0)" ::: "memory")
__device__ __forceinline__ f32x4 mfma(u32x4 a, u32x4 b, f32x4 c) {
  return __builtin_amdgcn_mfma_f32_16x16x32_bf16(__builtin_bit_cast(bf16x8, a), __builtin_bit_cast(bf16x8, b), c, 0, 0, 0);
}
__device__ __forceinline__ u32x4 bfrag(const u32x4* w, int e) {
  u32x4 o;
#pragma unroll
  for (int jj = 0; jj < 4; ++jj) o[jj] = __builtin_amdgcn_perm(w[2 * jj + 1][e >> 1], w[2 * jj][e >> 1], (e & 1) ? 0x07060302u : 0x05040100u);
  return o;
}
__device__ __forceinline__ u32x4 layernorm8(u32x4 xa) {
  float e[8];
#pragma unroll
  for (int k = 0; k < 4; ++k) { e[2 * k] = blo(xa[k]); e[2 * k + 1] = bhi(xa[k]); }
  const float mean = wave_sum(((e[0] + e[1]) + (e[2] + e[3])) + ((e[4] + e[5]) + (e[6] + e[7]))) * (1.0f / DM);
  float q = 0.f;
#pragma unroll
  for (int k = 0; k < 8; ++k) { const float d = e[k] - mean; q = fmaf(d, d, q); }
  const float var = wave_sum(q) * (1.0f / DM);
  const float mb = rbf(mean), sd = rbf(sqrtf(rbf(rbf(var) + 1e-5f)));
  const float ri = 1.0f / sd;
  u32x4 o;
#pragma unroll
  for (int k = 0; k < 4; ++k) {
    const float d0 = rbf(e[2 * k] - mb), d1 = rbf(e[2 * k + 1] - mb);
    float q0 = d0 * ri, q1 = d1 * ri;
    q0 = fmaf(fmaf(-q0, sd, d0), ri, q0); q1 = fmaf(fmaf(-q1, sd, d1), ri, q1);
    o[k] = pack2(q0, q1);
  }
  return o;
}

#define STAMP(i)
#define STAMP_DECL
#define STAMP_FLUSH

constexpr int L_QP = 0;
constexpr int L_A = L_QP + 4 * DH;
constexpr int L_CNT = L_A + 1024, L_TAG = L_CNT + 1, L_FW = L_CNT + 4;
constexpr int L_RED = L_CNT + 16;
constexpr int L_XN = L_RED + 128;
constexpr int L_GU = L_XN + 4 * 128;
constexpr int L_H = L_GU + 4 * 256;
constexpr int L_S = L_H + 4 * 64;
constexpr int L_TOT = L_S + 4 * NREVMAX * 64;

__global__ void __launch_bounds__(NTHR, 2) attn_kernel(const u16* __restrict__ kc, const u16* __restrict__ vc, const u16* __restrict__ x,
                                                       const u16* __restrict__ wq, unsigned* ws, u16* __restrict__ out,
                                                       const u16* __restrict__ wo, const u16* __restrict__ w2,
                                                       const u16* __restrict__ w1, const u16* __restrict__ w3) {
  __shared__ __attribute__((aligned(16))) unsigned lds[L_TOT];
  STAMP_DECL; STAMP(0);
  const int bid = blockIdx.x;
  const int tid = threadIdx.x, lane = tid & 63, wv = __builtin_amdgcn_readfirstlane(tid >> 6);
  const int j = bid >> 3, h = (bid & 7) * 4 + (j >> 3), b = (j >> 2) & 1, s = j & 3;
  const int r = lane >> 4, c = lane & 15;
  const bool fgrp = !(ABL & 2) && (bid & 1) == 0 && (j >> 2) < 7;
  const int w = wv - 4;
  const int fs = fgrp ? FSH : 0;
  int nst = NSTEP + (s == 0 ? -3 * fs : fs);
  int kvb = ((b * NH + h) * NSPLIT * NSTEP + s * NSTEP + (s == 0 ? 0 : (s - 4) * fs)) * STEPB;
  int so0 = w * 4096 + ((2 * s + b) * 8 + (s == 0 ? 0 : (4 - s) * fs)) * STEPB;
  asm volatile("" : "+s"(nst), "+s"(kvb), "+s"(so0) : "s"(kc), "s"(vc), "s"(x), "s"(wq), "s"(ws), "s"(out));

  if (wv < 4) {
    const int a = wv;
    __builtin_amdgcn_s_setprio(3);
    const rsrc_t rx = mkrsrc(x, NB * DM * 2);
    const u32x4 xa = bldc(rx, lane * 16, b * (DM * 2));
    u32x4 wr[32];
    const rsrc_t rq = mkrsrc(wq + h * DH, (DM * NH * DH - h * DH) * 2);
    const int vq = r * (8 * NH * DH * 2) + c * 16;
#define LD_WQ(i) { if ((i) >= WQ_INFL) asm volatile("s_waitcnt vmcnt(%0)" ::"n"(WQ_INFL - 1) : "memory"); \
      wr[i] = (ABL & 8) ? xa : bldc(rq, vq, (128 * a + 32 * ((((i) >> 3) + s) & 3) + ((i) & 7)) * (NH * DH * 2)); SB(); }
#pragma unroll
    for (int i = 0; i < WQ_INFL; ++i) LD_WQ(i)
    STAMP(1);
    if (a == 0) {
      const unsigned t = load_tag(ws);
      if (lane == 0) { lds[L_CNT] = 0u; lds[L_TAG] = t; }
    }
    SB();
    unsigned* xn = lds + L_XN + a * 128;
    {
      const u32x4 n0 = layernorm8(xa);
      if (r == a) *(u32x4*)(xn + 64 * b + 4 * c) = n0;
    }
    SB();
    STAMP(7);
    f32x4 d[8];
#pragma unroll
    for (int e = 0; e < 8; ++e) d[e] = f32x4{0.f, 0.f, 0.f, 0.f};
#pragma unroll
    for (int ks = 0; ks < 4; ++ks) {
#pragma unroll
      for (int i = 8 * ks + WQ_INFL; i < 8 * ks + 8 + WQ_INFL; ++i) if (i < 32) LD_WQ(i)
      const u32x4 af = *(const u32x4*)(xn + 64 * b + 16 * ((ks + s) & 3) + 4 * r);
#pragma unroll
      for (int e = 0; e < 8; ++e) d[e] = mfma(af, bfrag(wr + 8 * ks, e), d[e]);
      SB();
    }
#undef LD_WQ
    if (r == 0) {
      *(f32x4*)(lds + L_QP + a * DH + 8 * c) = f32x4{d[0][0], d[1][0], d[2][0], d[3][0]};
      *(f32x4*)(lds + L_QP + a * DH + 8 * c + 4) = f32x4{d[4][0], d[5][0], d[6][0], d[7][0]};
    }
    LGKM0();
    STAMP(6);
    __builtin_amdgcn_s_barrier();
    const unsigned tag = lds[L_TAG];
    const rsrc_t rw = mkrsrc(ws, WS_END);
    u32x4 bf[32];
    {
      const rsrc_t ro = mkrsrc(wo + (size_t)h * DH * DM, DH * DM * 2);
#pragma unroll
      for (int ks = 0; ks < 4; ++ks) {
#pragma unroll
        for (int i = 0; i < 8; ++i) {
          __builtin_amdgcn_s_sleep(WO_SLEEP);
          if (!(ABL & 1)) wr[i] = __builtin_amdgcn_raw_buffer_load_b128(ro, r * 8192 + c * 16 + (i & 3) * 1024, ks * 32768 + (i >> 2) * 4096 + a * 256, OAUX);
          SB();
        }
#pragma unroll
        for (int e = 0; e < 8; ++e) { bf[8 * ks + e] = bfrag(wr, e); asm volatile("" : "+v"(bf[8 * ks + e])); }
        SB();
      }
    }
    if (fgrp && s == 0) {
      const int sl = ((bid & 7) >> 1) * 7 + (j >> 2), f0 = sl * FSL;
      const u32x4 xo = bldc(rx, lane * 16, (b ^ 1) * (DM * 2));
      const GAS u32x4* p13 = (const GAS u32x4*)((c < 8 ? w1 : w3) + (size_t)(128 * a + 8 * r) * FF + f0 + 8 * (c & 7));
#pragma unroll
      for (int i = 0; i < 8; ++i) wr[i] = __builtin_nontemporal_load(p13 + i * (FF / 8));
      SB();
      {
        const u32x4 n1 = layernorm8(xo);
        if (r == a) *(u32x4*)(xn + 64 * (b ^ 1) + 4 * c) = n1;
      }
#pragma unroll
      for (int e = 0; e < 8; ++e) d[e] = f32x4{0.f, 0.f, 0.f, 0.f};
      const rsrc_t r2 = mkrsrc(w2 + (size_t)f0 * DM, FSL * DM * 2);
#pragma unroll
      for (int ks = 0; ks < 4; ++ks) {
        const u32x4 af = *(const u32x4*)(xn + 64 * (c & 1) + 16 * ks + 4 * r);
#pragma unroll
        for (int e = 0; e < 8; ++e) d[e] = mfma(af, bfrag(wr, e), d[e]);
        SB();
#pragma unroll
        for (int jj = 0; jj < 8; ++jj)
          wr[jj] = ks < 3 ? __builtin_nontemporal_load(p13 + (32 * (ks + 1) + jj) * (FF / 8))
                          : bld(r2, r * 8192 + c * 16 + (jj & 3) * 1024, (jj >> 2) * 4096 + a * 256);
        SB();
      }
      if (r < 2) {
        float* gu = (float*)lds + L_GU + a * 256 + r * 128 + 8 * c;
        *(f32x4*)(gu) = r ? f32x4{d[0][1], d[1][1], d[2][1], d[3][1]} : f32x4{d[0][0], d[1][0], d[2][0], d[3][0]};
        *(f32x4*)(gu + 4) = r ? f32x4{d[4][1], d[5][1], d[6][1], d[7][1]} : f32x4{d[4][0], d[5][0], d[6][0], d[7][0]};
      }
      LGKM0();
      if (lane == 0) __hip_atomic_fetch_add((LAS unsigned*)(lds + L_CNT), 1u, __ATOMIC_RELAXED, __HIP_MEMORY_SCOPE_WORKGROUP);
      while (*(volatile LAS unsigned*)(lds + L_CNT) < 4u) __builtin_amdgcn_s_sleep(1);
      asm volatile("" ::: "memory");
      {
        const float* gu = (const float*)lds + L_GU + (lane >> 5) * 128 + 2 * (lane & 31);
        f32x2 g = {0.f, 0.f}, u = {0.f, 0.f};
#pragma unroll
        for (int aa = 0; aa < 4; ++aa) { g += *(const f32x2*)(gu + aa * 256); u += *(const f32x2*)(gu + aa * 256 + 64); }
        float hv[2];
#pragma unroll
        for (int k = 0; k < 2; ++k) { const float gg = rbf(g[k]); hv[k] = rbf(gg / (1.0f + __expf(-gg))) * rbf(u[k]); }
        lds[L_H + a * 64 + lane] = pack2(hv[0], hv[1]);
      }
#pragma unroll
      for (int e = 0; e < 8; ++e) d[e] = f32x4{0.f, 0.f, 0.f, 0.f};
#pragma unroll
      for (int ks = 0; ks < 2; ++ks) {
        const u32x4 af = *(const u32x4*)(lds + L_H + a * 64 + 32 * (c & 1) + 16 * ks + 4 * r);
#pragma unroll
        for (int e = 0; e < 8; ++e) d[e] = mfma(af, bfrag(wr, e), d[e]);
        SB();
        if (ks == 0) {
#pragma unroll
          for (int jj = 0; jj < 8; ++jj) wr[jj] = bld(r2, r * 8192 + c * 16 + (jj & 3) * 1024, 32768 + (jj >> 2) * 4096 + a * 256);
          SB();
        }
      }
      {
        const int fo = WS_F + (((r & 1) * 32 + 8 * a + (c >> 1)) * NFW + sl) * 128 + (c & 1) * 64 + (r >> 1) * 32;
        float t[4];
#pragma unroll
        for (int e = 0; e < 4; ++e) {
          const float p0 = (r & 1) ? d[e][1] : d[e][0], p1 = (r & 1) ? d[4 + e][1] : d[4 + e][0];
          t[e] = r < 2 ? p0 : p1;
        }
        gst(rw, fo, 0, t[0], t[1], tag); gst(rw, fo + 16, 0, t[2], t[3], tag);
      }
      SB();
    }
    if (a == 0) {
      __builtin_amdgcn_s_sleep(ML_SLEEP);
      u32x4 ml;
      for (unsigned spin = 0; spin < SPIN_CAP; ++spin) {
        asm volatile("" ::: "memory");
        ml = gld(rw, c * 16, WS_ML + (b * NH + h) * 256);
        if (__builtin_amdgcn_ballot_w64(((ml[1] ^ tag) | (ml[3] ^ tag)) != 0u) == 0ull) break;
        __builtin_amdgcn_s_sleep(64);
      }
      const float mw = __uint_as_float(ml[0]), M = row16_max(mw);
      const float e = __builtin_amdgcn_exp2f(mw - M);
      const float f = e / row16_sum(e * __uint_as_float(ml[2]));
      if ((lane >> 2) == s) lds[L_FW + (lane & 3)] = __float_as_uint(f);
      LGKM0();
    }
    STAMP(4);
    __builtin_amdgcn_s_barrier();
    u32x4 af[4];
#pragma unroll
    for (int ks = 0; ks < 4; ++ks) af[ks] = *(const u32x4*)(lds + L_A + (ks * 64 + r * 16 + (c & 7)) * 4);
    const f32x2 fw = *(const f32x2*)(lds + L_FW + 2 * (r & 1));
    const float f0 = fw[0], f1 = fw[1];
#pragma unroll
    for (int e = 0; e < 8; ++e) d[e] = f32x4{0.f, 0.f, 0.f, 0.f};
#pragma unroll
    for (int ks = 0; ks < 4; ++ks)
#pragma unroll
      for (int e = 0; e < 8; ++e) d[e] = mfma(af[ks], bf[8 * ks + e], d[e]);
    float v[4];
#pragma unroll
    for (int e = 0; e < 4; ++e) {
      const float p0 = d[e][0] + d[e][1], p1 = d[e][2] + d[e][3], q0 = d[4 + e][0] + d[4 + e][1], q1 = d[4 + e][2] + d[4 + e][3];
      v[e] = f0 * (r < 2 ? p0 : q0) + f1 * (r < 2 ? p1 : q1);
    }
    gst(rw, (c >> 1) * (128 * 128) + (c & 1) * 64 + r * 16, WS_Y + ((b * 32 + 8 * a) * 128 + h * NSPLIT + s) * 128,
        t16_sum(v[0], v[2]), t16_sum(v[1], v[3]), tag);
    STAMP(3); STAMP_FLUSH;
    return;
  }

  u32x4 ring[16 * (1 + NXR)];
  const int vo = lane * 16;
  const int lim = nst * STEPB;
  const rsrc_t rk = mkrsrc((const char*)kc + (unsigned)kvb, lim), rv = mkrsrc((const char*)vc + (unsigned)kvb, lim);
  int so = so0;
#define ADV() so += STEPB; so = so >= lim ? so - lim : so;
#define FILL(rs, g, o) _Pragma("unroll") for (int i = 0; i < 4; ++i) { ring[(g) * 4 + i] = bld(rs, vo + i * 1024, o); SB(); }
#pragma unroll
  for (int g = 4; g < 4 + 4 * NXR; ++g) { FILL(rk, g, so) if (g == 4) STAMP(5); ADV() }
#pragma unroll
  for (int g = 0; g < 4; ++g) { FILL(rk, g, so) ADV() }
  STAMP(6);
  __builtin_amdgcn_s_barrier();
  unsigned qp[4];
  {
    f32x4 t0 = *(const f32x4*)(lds + L_QP + 8 * c), t1 = *(const f32x4*)(lds + L_QP + 8 * c + 4);
#pragma unroll
    for (int aa = 1; aa < 4; ++aa) {
      t0 += *(const f32x4*)(lds + L_QP + aa * DH + 8 * c);
      t1 += *(const f32x4*)(lds + L_QP + aa * DH + 8 * c + 4);
    }
    qp[0] = pack2(t0[0], t0[1]); qp[1] = pack2(t0[2], t0[3]); qp[2] = pack2(t1[0], t1[1]); qp[3] = pack2(t1[2], t1[3]);
  }
  STAMP(1);
  constexpr float SCALE = 0.088388347648318440f, LOG2E = 1.4426950408889634f;
  float* sl = (float*)lds + L_S + w * (NREVMAX * 64) + lane;
  float m = -INFINITY;
  float xs[16];
#define QK(g, slot)                                                                                  \
    _Pragma("unroll") for (int i = 0; i < 4; ++i) {                                                  \
      float d_ = 0.f;                                                                                \
      _Pragma("unroll") for (int k = 0; k < 4; ++k) d_ = dot2(ring[(slot) * 4 + i][k], qp[k], d_);   \
      xs[(g) * 4 + i] = d_;                                                                          \
    }                                                                                                \
    PIN4(xs + (g) * 4);
#define TR(HALF, CTRL) _Pragma("unroll") for (int k = 0; k < (HALF); ++k) {                          \
      const float t1_ = xs[k] + dpp<CTRL>(xs[k]), t2_ = xs[k + (HALF)] + dpp<CTRL>(xs[k + (HALF)]);  \
      xs[k] = (c & (HALF)) ? t2_ : t1_; }
#define SCORE(rev, nvalid)                                                                           \
  {                                                                                                  \
    TR(8, XOR8) TR(4, XOR7) TR(2, XOR2) TR(1, XOR1)                                                  \
    float t = rbf(rbf(xs[0]) * SCALE) * LOG2E;                     \
    t = (c >> 2) < (nvalid) ? t : -INFINITY;                                                         \
    m = fmaxf(m, t);                                                                                 \
    sl[(rev) * 64] = t;                                                                              \
    SB();                                                                                            \
  }
  const int nrev = (nst + 3) >> 2;
  {
    int left = nst - 4 * (1 + NXR);
    rsrc_t rn = rk;
#pragma nounroll
    for (int rev = NXR; rev < nrev; ++rev) {
      if (rev == nrev - 1) { rn = rv; so = so0; left = 4; }
#define STEP(g) { const int o_ = left > (g) ? so : lim; QK(g, g) FILL(rn, g, o_) ADV() }
      STEP(0) STEP(1) STEP(2) STEP(3)
#undef STEP
      SCORE(rev, nst - 4 * rev)
      left -= 4;
#define XREV(e) if constexpr ((e) < NXR) { if (rev == NXR + (e)) {                                   \
        _Pragma("unroll") for (int g = 0; g < 4; ++g) { PIN4(ring + (4 * (1 + (e)) + g) * 4); QK(g, 4 * (1 + (e)) + g) } \
        SCORE(e, 4) } }
      XREV(0) XREV(1)
#undef XREV
    }
  }
  {
    m = xrow_max(row16_max(m));
    float l = 0.f;
#pragma unroll
    for (int rev = 0; rev < NREVMAX; ++rev) {
      const float t = rev < NREVMAX - 3 || rev < nrev ? sl[rev * 64] : -INFINITY;
      const float p = __builtin_amdgcn_exp2f(t - m);
      l += p;
      sl[rev * 64] = p;
    }
    l = wave_sum(l);
    gst(mkrsrc(ws, WS_END), lane == 0 ? 0 : WS_END, WS_ML + (b * NH + h) * 256 + (4 * s + w) * 16, m, l, lds[L_TAG]);
    SB();
  }
  STAMP(3);
  f32x2 acc[4];
#pragma unroll
  for (int k = 0; k < 4; ++k) acc[k] = f32x2{0.f, 0.f};
#define PV1(g, i) { const float pb = dpp<BCAST + 4 * (g) + (i)>(p); const u32x4 vv = ring[(g) * 4 + (i)];  \
      _Pragma("unroll") for (int k = 0; k < 4; ++k)                                                  \
        acc[k] = __builtin_elementwise_fma(f32x2{pb, pb}, f32x2{blo(vv[k]), bhi(vv[k])}, acc[k]); }
#define PV(g) PV1(g, 0) PV1(g, 1) PV1(g, 2) PV1(g, 3) PIN4(acc);
  int left = nst - 4;
  float pn = sl[0];
#pragma nounroll
  for (int rev = 1; rev < nrev; ++rev) {
    const float p = pn;
    pn = sl[rev * 64];
#define STEP(g) { const int o_ = left > (g) ? so : lim; PV(g) FILL(rv, g, o_) ADV() }
    STEP(0) STEP(1) STEP(2) STEP(3)
#undef STEP
    left -= 4;
  }
  {
    const float p = pn;
    PV(0)
    if (left > -3) { PV(1) }
    if (left > -2) { PV(2) }
    if (left > -1) { PV(3) }
  }
  STAMP(2);
  {
    const float a0 = t32_sum(acc[0][0], acc[2][0]), a1 = t32_sum(acc[0][1], acc[2][1]);
    const float a2 = t32_sum(acc[1][0], acc[3][0]), a3 = t32_sum(acc[1][1], acc[3][1]);
    const float t0 = t16_sum(a0, a2), t1 = t16_sum(a1, a3);
    const unsigned hi = pack2(t0, t1), lo = pack2(t0 - blo(hi), t1 - bhi(hi));
    unsigned* pa = lds + L_A + ((c >> 2) * 64 + (c & 3) * 16 + 2 * w) * 4 + r;
    pa[0] = hi; pa[4] = lo;
  }
  LGKM0();
  STAMP(4);
  __builtin_amdgcn_s_barrier();
  if (!(ABL & 6) && (bid & 1) == CONS_PAR && j < 16) {
    const int ci = ((bid & 7) >> 1) * 16 + j, cb = ci >> 5, cg = ci & 31;
    const int t = tid - 256, pc = t & 7, hh = t >> 3;
    const unsigned tag = lds[L_TAG];
    const rsrc_t rw = mkrsrc(ws, WS_END);
    const int sy = WS_Y + (cb * 32 + cg) * (128 * 128);
    int vy = hh * 512 + pc * 16;
    int vf = hh < NFW ? WS_F + ((cb * 32 + cg) * NFW + hh) * 128 + pc * 16 : sy + vy;
    int vx = ci == 0 ? WS_ML + NH * 256 + (t & 127) * 64 : vf;
    const unsigned xv = *(const unsigned*)(x + cb * DM + cg * 16 + 2 * pc);
    float k0 = 0.f, k1 = 0.f, g0 = 0.f, g1 = 0.f, a0 = 0.f;
    bool done = false;
    u32x4 ya[5], yb[5];
    unsigned yat, ybt;
#define SWEEP(Y) { _Pragma("unroll") for (int i = 0; i < 4; ++i) Y[i] = gld(rw, vy + i * 128, sy);    \
      Y[4] = gld(rw, vf, 0); Y##t = __builtin_amdgcn_raw_buffer_load_b32(rw, vx, 4, 16); asm volatile("" ::: "memory"); SB(); }
#define CHECK(Y) {                                                                                   \
      unsigned bad = Y##t ^ tag;                                                                    \
      _Pragma("unroll") for (int i = 0; i < 5; ++i) bad |= (Y[i][1] ^ tag) | (Y[i][3] ^ tag);        \
      const bool take = !done && bad == 0u;                                                          \
      k0 = take ? (__uint_as_float(Y[0][0]) + __uint_as_float(Y[1][0])) + (__uint_as_float(Y[2][0]) + __uint_as_float(Y[3][0])) : k0; \
      k1 = take ? (__uint_as_float(Y[0][2]) + __uint_as_float(Y[1][2])) + (__uint_as_float(Y[2][2]) + __uint_as_float(Y[3][2])) : k1; \
      g0 = take && hh < NFW ? __uint_as_float(Y[4][0]) : g0; g1 = take && hh < NFW ? __uint_as_float(Y[4][2]) : g1; \
      done = done || take;                                                                           \
      vy = done ? WS_END : vy; vf = done ? WS_END : vf; vx = done ? WS_END : vx;                     \
      a0 = t32_sum(k0 + dpp<XOR8>(k0), g0 + dpp<XOR8>(g0));        \
      a0 = t16_sum(a0, t32_sum(k1 + dpp<XOR8>(k1), g1 + dpp<XOR8>(g1)));     \
      const unsigned long long miss = __builtin_amdgcn_ballot_w64(!done);                            \
      if (miss == 0ull) break;                                                                       \
      if (__builtin_popcountll(miss) > POLL_MANY) __builtin_amdgcn_s_sleep(POLL_SLEEP);              \
      asm volatile("" ::: "memory"); }
    for (unsigned spin = 0; spin < SPIN_CAP; ++spin) { SWEEP(ya) CHECK(ya) }
#undef SWEEP
#undef CHECK
    if (c < 8) lds[L_RED + w * 32 + (r >> 1) * 16 + 2 * c + (r & 1)] = __float_as_uint(a0);
    LGKM0();
    unsigned tk = 0;
    if (lane == 0) tk = __hip_atomic_fetch_add((LAS unsigned*)(lds + L_CNT), 16u, __ATOMIC_RELAXED, __HIP_MEMORY_SCOPE_WORKGROUP);
    if (__builtin_amdgcn_readfirstlane(tk) >= 48u) {
      asm volatile("" ::: "memory");
      if (lane < 8) {
        const float* red = (const float*)lds + L_RED + 2 * lane;
        const f32x2 A = (*(const f32x2*)(red) + *(const f32x2*)(red + 32)) + (*(const f32x2*)(red + 64) + *(const f32x2*)(red + 96));
        const f32x2 F = (*(const f32x2*)(red + 16) + *(const f32x2*)(red + 48)) + (*(const f32x2*)(red + 80) + *(const f32x2*)(red + 112));
        *(unsigned*)(out + cb * DM + cg * 16 + 2 * lane) = pack2(rbf(blo(xv) + rbf(A[0])) + rbf(F[0]), rbf(bhi(xv) + rbf(A[1])) + rbf(F[1]));
      }
      if (ci == 0 && lane == 0) __builtin_amdgcn_raw_buffer_store_b32(tag, rw, WS_E, 0, 16);
    }
  }
  STAMP(7);
  STAMP_FLUSH;
}

}

extern "C" void kernel_launch(void* const* d_in, const int* in_sizes, int n_in, void* d_out, int out_size, void* d_ws,
                              size_t ws_size, hipStream_t stream) {
  fprintf(stderr, "PTRS x=%p K=%p V=%p wq=%p wo=%p w1=%p w3=%p w2=%p out=%p ws=%p\n", d_in[0], d_in[1], d_in[2], d_in[3], d_in[4], d_in[6], d_in[7], d_in[8], d_out, d_ws);
  attn_kernel<<<NWG, NTHR, 0, stream>>>((const u16*)d_in[1], (const u16*)d_in[2], (const u16*)d_in[0], (const u16*)d_in[3], (unsigned*)d_ws,
                                        (u16*)d_out, (const u16*)d_in[4], (const u16*)d_in[8], (const u16*)d_in[6], (const u16*)d_in[7]);
}
```

```cpp
#include <hip/hip_runtime.h>
#include <hip/hip_bf16.h>
#include <cstdint>

#define WQ_INFL 8
#define WO_SLEEP 24
#define CONS_PAR 0
#define POLL_MANY 32
#define POLL_SLEEP 16
#define OAUX 2
#define PHASE_FIX 1
#define ABL 0
#define ML_SLEEP 3
#define PIPE 0
#define PRETOUCH 0
#define FSH 2
#define NXR 2

namespace {

constexpr int NB = 2, DM = 512, NH = 32, SK = 16384, DH = 128, FF = 1792;
constexpr int NSPLIT = 4;
constexpr int NTHR = 512;
constexpr int NWG = NB * NH * NSPLIT;
constexpr int STEPK = 64, STEPB = STEPK * DH * 2;
constexpr int NSTEP = SK / NSPLIT / STEPK;
constexpr int NREVMAX = (NSTEP + FSH + 3) / 4;
constexpr int FSL = 64, NFW = FF / FSL;

constexpr int WS_E = 0;
constexpr int WS_ML = 4096;
constexpr int WS_F = WS_ML + NB * NH * 256;
constexpr int WS_Y = WS_F + NB * 32 * NFW * 128;
constexpr int WS_END = WS_Y + NB * 32 * 128 * 128;
constexpr unsigned SPIN_CAP = 1u << 20;

typedef unsigned u32x4 __attribute__((ext_vector_type(4)));
typedef float f32x4 __attribute__((ext_vector_type(4)));
typedef float f32x2 __attribute__((ext_vector_type(2)));
typedef __bf16 bf16x2 __attribute__((ext_vector_type(2)));
typedef __bf16 bf16x8 __attribute__((ext_vector_type(8)));
typedef unsigned short u16;
#define GAS __attribute__((address_space(1)))
#define LAS __attribute__((address_space(3)))

__device__ __forceinline__ float dot2(unsigned a, unsigned b, float acc) {
  return __builtin_amdgcn_fdot2_f32_bf16(__builtin_bit_cast(bf16x2, a), __builtin_bit_cast(bf16x2, b), acc, false);
}
__device__ __forceinline__ float blo(unsigned u) { return __builtin_bit_cast(float, u << 16); }
__device__ __forceinline__ float bhi(unsigned u) { return __builtin_bit_cast(float, u & 0xffff0000u); }
__device__ __forceinline__ float rbf(float f) { return (float)(__bf16)f; }
__device__ __forceinline__ unsigned pack2(float lo, float hi) {
  bf16x2 v; v.x = (__bf16)lo; v.y = (__bf16)hi; return __builtin_bit_cast(unsigned, v);
}
template <int CTRL> __device__ __forceinline__ float dpp(float x) {
  return __builtin_bit_cast(float, __builtin_amdgcn_mov_dpp(__builtin_bit_cast(int, x), CTRL, 0xf, 0xf, true));
}
constexpr int XOR1 = 0xB1, XOR2 = 0x4E, XOR7 = 0x141, XOR8 = 0x128, BCAST = 0x150;
__device__ __forceinline__ float row16_sum(float x) {
  x += dpp<XOR1>(x); x += dpp<XOR2>(x); x += dpp<XOR7>(x); x += dpp<XOR8>(x); return x;
}
__device__ __forceinline__ float row16_max(float x) {
  x = fmaxf(x, dpp<XOR1>(x)); x = fmaxf(x, dpp<XOR2>(x)); x = fmaxf(x, dpp<XOR7>(x)); return fmaxf(x, dpp<XOR8>(x));
}
__device__ __forceinline__ float t16_sum(float a, float b) {
  auto s = __builtin_amdgcn_permlane16_swap(__float_as_uint(a), __float_as_uint(b), false, false);
  return __uint_as_float(s[0]) + __uint_as_float(s[1]);
}
__device__ __forceinline__ float t32_sum(float a, float b) {
  auto s = __builtin_amdgcn_permlane32_swap(__float_as_uint(a), __float_as_uint(b), false, false);
  return __uint_as_float(s[0]) + __uint_as_float(s[1]);
}
__device__ __forceinline__ float xrow_sum(float x) { x = t16_sum(x, x); return t32_sum(x, x); }
__device__ __forceinline__ float xrow_max(float x) {
  auto s = __builtin_amdgcn_permlane16_swap(__float_as_uint(x), __float_as_uint(x), false, false);
  x = fmaxf(__uint_as_float(s[0]), __uint_as_float(s[1]));
  auto t = __builtin_amdgcn_permlane32_swap(__float_as_uint(x), __float_as_uint(x), false, false);
  return fmaxf(__uint_as_float(t[0]), __uint_as_float(t[1]));
}
__device__ __forceinline__ float wave_sum(float x) { return xrow_sum(row16_sum(x)); }
typedef __amdgpu_buffer_rsrc_t rsrc_t;
__device__ __forceinline__ rsrc_t mkrsrc(const void* p, int bytes) { return __builtin_amdgcn_make_buffer_rsrc((void*)p, 0, bytes, 0x00020000); }
__device__ __forceinline__ u32x4 bld(rsrc_t r, int voff, int soff) { return __builtin_amdgcn_raw_buffer_load_b128(r, voff, soff, 2); }
__device__ __forceinline__ u32x4 bldc(rsrc_t r, int voff, int soff) { return __builtin_amdgcn_raw_buffer_load_b128(r, voff, soff, 0); }
__device__ __forceinline__ u32x4 gld(rsrc_t r, int voff, int soff) { return __builtin_amdgcn_raw_buffer_load_b128(r, voff, soff, 16); }
__device__ __forceinline__ void gst(rsrc_t r, int voff, int soff, float v0, float v1, unsigned tag) {
  __builtin_amdgcn_raw_buffer_store_b128(u32x4{__float_as_uint(v0), tag, __float_as_uint(v1), tag}, r, voff, soff, 16);
}
#define SB() __builtin_amdgcn_sched_barrier(0)
__device__ __forceinline__ unsigned load_tag(const unsigned* ws) {
  unsigned e;
  asm volatile("s_load_dword %0, %1, 0x0\n\ts_waitcnt lgkmcnt(0)" : "=s"(e) : "s"(ws) : "memory");
  return e + 1u;
}
#define PIN4(a) asm volatile("" : "+v"((a)[0]), "+v"((a)[1]), "+v"((a)[2]), "+v"((a)[3])::"memory"); SB()
#define LGKM0() asm volatile("s_waitcnt lgkmcnt(0)" ::: "memory")
__device__ __forceinline__ f32x4 mfma(u32x4 a, u32x4 b, f32x4 c) {
  return __builtin_amdgcn_mfma_f32_16x16x32_bf16(__builtin_bit_cast(bf16x8, a), __builtin_bit_cast(bf16x8, b), c, 0, 0, 0);
}
__device__ __forceinline__ u32x4 bfrag(const u32x4* w, int e) {
  u32x4 o;
#pragma unroll
  for (int jj = 0; jj < 4; ++jj) o[jj] = __builtin_amdgcn_perm(w[2 * jj + 1][e >> 1], w[2 * jj][e >> 1], (e & 1) ? 0x07060302u : 0x05040100u);
  return o;
}
__device__ __forceinline__ u32x4 layernorm8(u32x4 xa) {
  float e[8];
#pragma unroll
  for (int k = 0; k < 4; ++k) { e[2 * k] = blo(xa[k]); e[2 * k + 1] = bhi(xa[k]); }
  const float mean = wave_sum(((e[0] + e[1]) + (e[2] + e[3])) + ((e[4] + e[5]) + (e[6] + e[7]))) * (1.0f / DM);
  float q = 0.f;
#pragma unroll
  for (int k = 0; k < 8; ++k) { const float d = e[k] - mean; q = fmaf(d, d, q); }
  const float var = wave_sum(q) * (1.0f / DM);
  const float mb = rbf(mean), sd = rbf(sqrtf(rbf(rbf(var) + 1e-5f)));
  const float ri = 1.0f / sd;
  u32x4 o;
#pragma unroll
  for (int k = 0; k < 4; ++k) {
    const float d0 = rbf(e[2 * k] - mb), d1 = rbf(e[2 * k + 1] - mb);
    float q0 = d0 * ri, q1 = d1 * ri;
    q0 = fmaf(fmaf(-q0, sd, d0), ri, q0); q1 = fmaf(fmaf(-q1, sd, d1), ri, q1);
    o[k] = pack2(q0, q1);
  }
  return o;
}

#define STAMP(i)
#define STAMP_DECL
#define STAMP_FLUSH

constexpr int L_QP = 0;
constexpr int L_A = L_QP + 4 * DH;
constexpr int L_CNT = L_A + 1024, L_TAG = L_CNT + 1, L_MID = L_CNT + 2, L_FW = L_CNT + 4;
constexpr int L_RED = L_CNT + 16;
constexpr int L_XN = L_RED + 128;
constexpr int L_GU = L_XN + 4 * 128;
constexpr int L_H = L_GU + 4 * 256;
constexpr int L_S = L_H + 4 * 64;
constexpr int L_TOT = L_S + 4 * NREVMAX * 64;

__global__ void __launch_bounds__(NTHR, 2) attn_kernel(const u16* __restrict__ kc, const u16* __restrict__ vc, const u16* __restrict__ x,
                                                       const u16* __restrict__ wq, unsigned* ws, u16* __restrict__ out,
                                                       const u16* __restrict__ wo, const u16* __restrict__ w2,
                                                       const u16* __restrict__ w1, const u16* __restrict__ w3) {
  __shared__ __attribute__((aligned(16))) unsigned lds[L_TOT];
  STAMP_DECL; STAMP(0);
  const int bid = blockIdx.x;
  const int tid = threadIdx.x, lane = tid & 63, wv = __builtin_amdgcn_readfirstlane(tid >> 6);
  const int j = bid >> 3, h = (bid & 7) * 4 + (j >> 3), b = (j >> 2) & 1, s = j & 3;
  const int r = lane >> 4, c = lane & 15;
  const bool fgrp = !(ABL & 2) && (bid & 1) == 0 && (j >> 2) < 7;
  const int w = wv - 4;
  const int fs = fgrp ? FSH : 0;
  int nst = NSTEP + (s == 0 ? -3 * fs : fs);
  int kvb = ((b * NH + h) * NSPLIT * NSTEP + s * NSTEP + (s == 0 ? 0 : (s - 4) * fs)) * STEPB;
  int so0 = w * 4096 + ((2 * s + b) * 8 + (s == 0 ? 0 : (4 - s) * fs)) * STEPB;
  asm volatile("" : "+s"(nst), "+s"(kvb), "+s"(so0) : "s"(kc), "s"(vc), "s"(x), "s"(wq), "s"(ws), "s"(out));

  if (wv < 4) {
    const int a = wv;
    __builtin_amdgcn_s_setprio(3);
    const rsrc_t rx = mkrsrc(x, NB * DM * 2);
    const u32x4 xa = bldc(rx, lane * 16, b * (DM * 2));
    u32x4 wr[32];
    const rsrc_t rq = mkrsrc(wq + h * DH, (DM * NH * DH - h * DH) * 2);
    const int vq = r * (8 * NH * DH * 2) + c * 16;
#define LD_WQ(i) { if ((i) >= WQ_INFL) asm volatile("s_waitcnt vmcnt(%0)" ::"n"(WQ_INFL - 1) : "memory"); \
      wr[i] = (ABL & 8) ? xa : bldc(rq, vq, (128 * a + 32 * ((((i) >> 3) + s) & 3) + ((i) & 7)) * (NH * DH * 2)); SB(); }
#pragma unroll
    for (int i = 0; i < WQ_INFL; ++i) LD_WQ(i)
    STAMP(1);
    if (a == 0) {
      const unsigned t = load_tag(ws);
      if (lane == 0) { lds[L_CNT] = 0u; lds[L_TAG] = t; lds[L_MID] = 0u; }
    }
    SB();
    unsigned* xn = lds + L_XN + a * 128;
    {
      const u32x4 n0 = layernorm8(xa);
      if (r == a) *(u32x4*)(xn + 64 * b + 4 * c) = n0;
    }
    SB();
    STAMP(7);
    f32x4 d[8];
#pragma unroll
    for (int e = 0; e < 8; ++e) d[e] = f32x4{0.f, 0.f, 0.f, 0.f};
#pragma unroll
    for (int ks = 0; ks < 4; ++ks) {
#pragma unroll
      for (int i = 8 * ks + WQ_INFL; i < 8 * ks + 8 + WQ_INFL; ++i) if (i < 32) LD_WQ(i)
      const u32x4 af = *(const u32x4*)(xn + 64 * b + 16 * ((ks + s) & 3) + 4 * r);
#pragma unroll
      for (int e = 0; e < 8; ++e) d[e] = mfma(af, bfrag(wr + 8 * ks, e), d[e]);
      SB();
    }
#undef LD_WQ
    if (r == 0) {
      *(f32x4*)(lds + L_QP + a * DH + 8 * c) = f32x4{d[0][0], d[1][0], d[2][0], d[3][0]};
      *(f32x4*)(lds + L_QP + a * DH + 8 * c + 4) = f32x4{d[4][0], d[5][0], d[6][0], d[7][0]};
    }
    LGKM0();
    STAMP(6);
    __builtin_amdgcn_s_barrier();
    const unsigned tag = lds[L_TAG];
    const rsrc_t rw = mkrsrc(ws, WS_END);
    u32x4 bf[32];
    {
      const rsrc_t ro = mkrsrc(wo + (size_t)h * DH * DM, DH * DM * 2);
#pragma unroll
      for (int ks = 0; ks < 4; ++ks) {
#pragma unroll
        for (int i = 0; i < 8; ++i) {
          __builtin_amdgcn_s_sleep(WO_SLEEP);
          if (!(ABL & 1)) wr[i] = __builtin_amdgcn_raw_buffer_load_b128(ro, r * 8192 + c * 16 + (i & 3) * 1024, ks * 32768 + (i >> 2) * 4096 + a * 256, OAUX);
          SB();
        }
#pragma unroll
        for (int e = 0; e < 8; ++e) { bf[8 * ks + e] = bfrag(wr, e); asm volatile("" : "+v"(bf[8 * ks + e])); }
        SB();
      }
    }
    if (fgrp && s == 0) {
      const int sl = ((bid & 7) >> 1) * 7 + (j >> 2), f0 = sl * FSL;
      const u32x4 xo = bldc(rx, lane * 16, (b ^ 1) * (DM * 2));
      const GAS u32x4* p13 = (const GAS u32x4*)((c < 8 ? w1 : w3) + (size_t)(128 * a + 8 * r) * FF + f0 + 8 * (c & 7));
#pragma unroll
      for (int i = 0; i < 8; ++i) wr[i] = __builtin_nontemporal_load(p13 + i * (FF / 8));
      SB();
      {
        const u32x4 n1 = layernorm8(xo);
        if (r == a) *(u32x4*)(xn + 64 * (b ^ 1) + 4 * c) = n1;
      }
#pragma unroll
      for (int e = 0; e < 8; ++e) d[e] = f32x4{0.f, 0.f, 0.f, 0.f};
      const rsrc_t r2 = mkrsrc(w2 + (size_t)f0 * DM, FSL * DM * 2);
#pragma unroll
      for (int ks = 0; ks < 4; ++ks) {
        const u32x4 af = *(const u32x4*)(xn + 64 * (c & 1) + 16 * ks + 4 * r);
#pragma unroll
        for (int e = 0; e < 8; ++e) d[e] = mfma(af, bfrag(wr, e), d[e]);
        SB();
#pragma unroll
        for (int jj = 0; jj < 8; ++jj)
          wr[jj] = ks < 3 ? __builtin_nontemporal_load(p13 + (32 * (ks + 1) + jj) * (FF / 8))
                          : bld(r2, r * 8192 + c * 16 + (jj & 3) * 1024, (jj >> 2) * 4096 + a * 256);
        SB();
      }
      if (r < 2) {
        float* gu = (float*)lds + L_GU + a * 256 + r * 128 + 8 * c;
        *(f32x4*)(gu) = r ? f32x4{d[0][1], d[1][1], d[2][1], d[3][1]} : f32x4{d[0][0], d[1][0], d[2][0], d[3][0]};
        *(f32x4*)(gu + 4) = r ? f32x4{d[4][1], d[5][1], d[6][1], d[7][1]} : f32x4{d[4][0], d[5][0], d[6][0], d[7][0]};
      }
      LGKM0();
      if (lane == 0) __hip_atomic_fetch_add((LAS unsigned*)(lds + L_CNT), 1u, __ATOMIC_RELAXED, __HIP_MEMORY_SCOPE_WORKGROUP);
      while (*(volatile LAS unsigned*)(lds + L_CNT) < 4u) __builtin_amdgcn_s_sleep(1);
      asm volatile("" ::: "memory");
      {
        const float* gu = (const float*)lds + L_GU + (lane >> 5) * 128 + 2 * (lane & 31);
        f32x2 g = {0.f, 0.f}, u = {0.f, 0.f};
#pragma unroll
        for (int aa = 0; aa < 4; ++aa) { g += *(const f32x2*)(gu + aa * 256); u += *(const f32x2*)(gu + aa * 256 + 64); }
        float hv[2];
#pragma unroll
        for (int k = 0; k < 2; ++k) { const float gg = rbf(g[k]); hv[k] = rbf(gg / (1.0f + __expf(-gg))) * rbf(u[k]); }
        lds[L_H + a * 64 + lane] = pack2(hv[0], hv[1]);
      }
#pragma unroll
      for (int e = 0; e < 8; ++e) d[e] = f32x4{0.f, 0.f, 0.f, 0.f};
#pragma unroll
      for (int ks = 0; ks < 2; ++ks) {
        const u32x4 af = *(const u32x4*)(lds + L_H + a * 64 + 32 * (c & 1) + 16 * ks + 4 * r);
#pragma unroll
        for (int e = 0; e < 8; ++e) d[e] = mfma(af, bfrag(wr, e), d[e]);
        SB();
        if (ks == 0) {
#pragma unroll
          for (int jj = 0; jj < 8; ++jj) wr[jj] = bld(r2, r * 8192 + c * 16 + (jj & 3) * 1024, 32768 + (jj >> 2) * 4096 + a * 256);
          SB();
        }
      }
      {
        const int fo = WS_F + (((r & 1) * 32 + 8 * a + (c >> 1)) * NFW + sl) * 128 + (c & 1) * 64 + (r >> 1) * 32;
        float t[4];
#pragma unroll
        for (int e = 0; e < 4; ++e) {
          const float p0 = (r & 1) ? d[e][1] : d[e][0], p1 = (r & 1) ? d[4 + e][1] : d[4 + e][0];
          t[e] = r < 2 ? p0 : p1;
        }
        gst(rw, fo, 0, t[0], t[1], tag); gst(rw, fo + 16, 0, t[2], t[3], tag);
      }
      SB();
    }
    if (a == 0) {
      while (*(volatile LAS unsigned*)(lds + L_MID) < 4u) __builtin_amdgcn_s_sleep(16);
      if (!(fgrp && s == 0)) for (int i = 0; i < ML_SLEEP; ++i) __builtin_amdgcn_s_sleep(127);
      u32x4 ml;
      for (unsigned spin = 0; spin < SPIN_CAP; ++spin) {
        asm volatile("" ::: "memory");
        ml = gld(rw, c * 16, WS_ML + (b * NH + h) * 256);
        if (__builtin_amdgcn_ballot_w64(((ml[1] ^ tag) | (ml[3] ^ tag)) != 0u) == 0ull) break;
        __builtin_amdgcn_s_sleep(64);
      }
      const float mw = __uint_as_float(ml[0]), M = row16_max(mw);
      const float e = __builtin_amdgcn_exp2f(mw - M);
      const float f = e / row16_sum(e * __uint_as_float(ml[2]));
      if ((lane >> 2) == s) lds[L_FW + (lane & 3)] = __float_as_uint(f);
      LGKM0();
    }
    STAMP(4);
    __builtin_amdgcn_s_barrier();
    u32x4 af[4];
#pragma unroll
    for (int ks = 0; ks < 4; ++ks) af[ks] = *(const u32x4*)(lds + L_A + (ks * 64 + r * 16 + (c & 7)) * 4);
    const f32x2 fw = *(const f32x2*)(lds + L_FW + 2 * (r & 1));
    const float f0 = fw[0], f1 = fw[1];
#pragma unroll
    for (int e = 0; e < 8; ++e) d[e] = f32x4{0.f, 0.f, 0.f, 0.f};
#pragma unroll
    for (int ks = 0; ks < 4; ++ks)
#pragma unroll
      for (int e = 0; e < 8; ++e) d[e] = mfma(af[ks], bf[8 * ks + e], d[e]);
    float v[4];
#pragma unroll
    for (int e = 0; e < 4; ++e) {
      const float p0 = d[e][0] + d[e][1], p1 = d[e][2] + d[e][3], q0 = d[4 + e][0] + d[4 + e][1], q1 = d[4 + e][2] + d[4 + e][3];
      v[e] = f0 * (r < 2 ? p0 : q0) + f1 * (r < 2 ? p1 : q1);
    }
    gst(rw, (c >> 1) * (128 * 128) + (c & 1) * 64 + r * 16, WS_Y + ((b * 32 + 8 * a) * 128 + h * NSPLIT + s) * 128,
        t16_sum(v[0], v[2]), t16_sum(v[1], v[3]), tag);
    STAMP(3); STAMP_FLUSH;
    return;
  }

  u32x4 ring[16 * (1 + NXR)];
  const int vo = lane * 16;
  const int lim = nst * STEPB;
  const rsrc_t rk = mkrsrc((const char*)kc + (unsigned)kvb, lim), rv = mkrsrc((const char*)vc + (unsigned)kvb, lim);
  int so = so0;
#define ADV() so += STEPB; so = so >= lim ? so - lim : so;
#define FILL(rs, g, o) _Pragma("unroll") for (int i = 0; i < 4; ++i) { ring[(g) * 4 + i] = bld(rs, vo + i * 1024, o); SB(); }
#pragma unroll
  for (int g = 4; g < 4 + 4 * NXR; ++g) { FILL(rk, g, so) if (g == 4) STAMP(5); ADV() }
#pragma unroll
  for (int g = 0; g < 4; ++g) { FILL(rk, g, so) ADV() }
  STAMP(6);
  __builtin_amdgcn_s_barrier();
  unsigned qp[4];
  {
    f32x4 t0 = *(const f32x4*)(lds + L_QP + 8 * c), t1 = *(const f32x4*)(lds + L_QP + 8 * c + 4);
#pragma unroll
    for (int aa = 1; aa < 4; ++aa) {
      t0 += *(const f32x4*)(lds + L_QP + aa * DH + 8 * c);
      t1 += *(const f32x4*)(lds + L_QP + aa * DH + 8 * c + 4);
    }
    qp[0] = pack2(t0[0], t0[1]); qp[1] = pack2(t0[2], t0[3]); qp[2] = pack2(t1[0], t1[1]); qp[3] = pack2(t1[2], t1[3]);
  }
  STAMP(1);
  constexpr float SCALE = 0.088388347648318440f, LOG2E = 1.4426950408889634f;
  float* sl = (float*)lds + L_S + w * (NREVMAX * 64) + lane;
  float m = -INFINITY;
  float xs[16];
#define QK(g, slot)                                                                                  \
    _Pragma("unroll") for (int i = 0; i < 4; ++i) {                                                  \
      float d_ = 0.f;                                                                                \
      _Pragma("unroll") for (int k = 0; k < 4; ++k) d_ = dot2(ring[(slot) * 4 + i][k], qp[k], d_);   \
      xs[(g) * 4 + i] = d_;                                                                          \
    }                                                                                                \
    PIN4(xs + (g) * 4);
#define TR(HALF, CTRL) _Pragma("unroll") for (int k = 0; k < (HALF); ++k) {                          \
      const float t1_ = xs[k] + dpp<CTRL>(xs[k]), t2_ = xs[k + (HALF)] + dpp<CTRL>(xs[k + (HALF)]);  \
      xs[k] = (c & (HALF)) ? t2_ : t1_; }
#define SCORE(rev, nvalid)                                                                           \
  {                                                                                                  \
    TR(8, XOR8) TR(4, XOR7) TR(2, XOR2) TR(1, XOR1)                                                  \
    float t = rbf(rbf(xs[0]) * SCALE) * LOG2E;                     \
    t = (c >> 2) < (nvalid) ? t : -INFINITY;                                                         \
    m = fmaxf(m, t);                                                                                 \
    sl[(rev) * 64] = t;                                                                              \
    SB();                                                                                            \
  }
  const int nrev = (nst + 3) >> 2;
  {
    int left = nst - 4 * (1 + NXR);
    rsrc_t rn = rk;
#pragma nounroll
    for (int rev = NXR; rev < nrev; ++rev) {
      if (rev == nrev - 1) { rn = rv; so = so0; left = 4; }
#define STEP(g) { const int o_ = left > (g) ? so : lim; QK(g, g) FILL(rn, g, o_) ADV() }
      STEP(0) STEP(1) STEP(2) STEP(3)
#undef STEP
      SCORE(rev, nst - 4 * rev)
      left -= 4;
#define XREV(e) if constexpr ((e) < NXR) { if (rev == NXR + (e)) {                                   \
        _Pragma("unroll") for (int g = 0; g < 4; ++g) { PIN4(ring + (4 * (1 + (e)) + g) * 4); QK(g, 4 * (1 + (e)) + g) } \
        SCORE(e, 4) } }
      XREV(0) XREV(1)
#undef XREV
    }
  }
  {
    m = xrow_max(row16_max(m));
    float l = 0.f;
#pragma unroll
    for (int rev = 0; rev < NREVMAX; ++rev) {
      const float t = rev < NREVMAX - 3 || rev < nrev ? sl[rev * 64] : -INFINITY;
      const float p = __builtin_amdgcn_exp2f(t - m);
      l += p;
      sl[rev * 64] = p;
    }
    l = wave_sum(l);
    gst(mkrsrc(ws, WS_END), lane == 0 ? 0 : WS_END, WS_ML + (b * NH + h) * 256 + (4 * s + w) * 16, m, l, lds[L_TAG]);
    if (lane == 0) __hip_atomic_fetch_add((LAS unsigned*)(lds + L_MID), 1u, __ATOMIC_RELAXED, __HIP_MEMORY_SCOPE_WORKGROUP);
    SB();
  }
  STAMP(3);
  f32x2 acc[4];
#pragma unroll
  for (int k = 0; k < 4; ++k) acc[k] = f32x2{0.f, 0.f};
#define PV1(g, i) { const float pb = dpp<BCAST + 4 * (g) + (i)>(p); const u32x4 vv = ring[(g) * 4 + (i)];  \
      _Pragma("unroll") for (int k = 0; k < 4; ++k)                                                  \
        acc[k] = __builtin_elementwise_fma(f32x2{pb, pb}, f32x2{blo(vv[k]), bhi(vv[k])}, acc[k]); }
#define PV(g) PV1(g, 0) PV1(g, 1) PV1(g, 2) PV1(g, 3) PIN4(acc);
  int left = nst - 4;
  float pn = sl[0];
#pragma nounroll
  for (int rev = 1; rev < nrev; ++rev) {
    const float p = pn;
    pn = sl[rev * 64];
#define STEP(g) { const int o_ = left > (g) ? so : lim; PV(g) FILL(rv, g, o_) ADV() }
    STEP(0) STEP(1) STEP(2) STEP(3)
#undef STEP
    left -= 4;
  }
  {
    const float p = pn;
    PV(0)
    if (left > -3) { PV(1) }
    if (left > -2) { PV(2) }
    if (left > -1) { PV(3) }
  }
  STAMP(2);
  {
    const float a0 = t32_sum(acc[0][0], acc[2][0]), a1 = t32_sum(acc[0][1], acc[2][1]);
    const float a2 = t32_sum(acc[1][0], acc[3][0]), a3 = t32_sum(acc[1][1], acc[3][1]);
    const float t0 = t16_sum(a0, a2), t1 = t16_sum(a1, a3);
    const unsigned hi = pack2(t0, t1), lo = pack2(t0 - blo(hi), t1 - bhi(hi));
    unsigned* pa = lds + L_A + ((c >> 2) * 64 + (c & 3) * 16 + 2 * w) * 4 + r;
    pa[0] = hi; pa[4] = lo;
  }
  LGKM0();
  STAMP(4);
  __builtin_amdgcn_s_barrier();
  if (!(ABL & 6) && (bid & 1) == CONS_PAR && j < 16) {
    const int ci = ((bid & 7) >> 1) * 16 + j, cb = ci >> 5, cg = ci & 31;
    const int t = tid - 256, pc = t & 7, hh = t >> 3;
    const unsigned tag = lds[L_TAG];
    const rsrc_t rw = mkrsrc(ws, WS_END);
    const int sy = WS_Y + (cb * 32 + cg) * (128 * 128);
    int vy = hh * 512 + pc * 16;
    int vf = hh < NFW ? WS_F + ((cb * 32 + cg) * NFW + hh) * 128 + pc * 16 : sy + vy;
    int vx = ci == 0 ? WS_ML + NH * 256 + (t & 127) * 64 : vf;
    const unsigned xv = *(const unsigned*)(x + cb * DM + cg * 16 + 2 * pc);
    float k0 = 0.f, k1 = 0.f, g0 = 0.f, g1 = 0.f, a0 = 0.f;
    bool done = false;
    u32x4 ya[5], yb[5];
    unsigned yat, ybt;
#define SWEEP(Y) { _Pragma("unroll") for (int i = 0; i < 4; ++i) Y[i] = gld(rw, vy + i * 128, sy);    \
      Y[4] = gld(rw, vf, 0); Y##t = __builtin_amdgcn_raw_buffer_load_b32(rw, vx, 4, 16); asm volatile("" ::: "memory"); SB(); }
#define CHECK(Y) {                                                                                   \
      unsigned bad = Y##t ^ tag;                                                                    \
      _Pragma("unroll") for (int i = 0; i < 5; ++i) bad |= (Y[i][1] ^ tag) | (Y[i][3] ^ tag);        \
      const bool take = !done && bad == 0u;                                                          \
      k0 = take ? (__uint_as_float(Y[0][0]) + __uint_as_float(Y[1][0])) + (__uint_as_float(Y[2][0]) + __uint_as_float(Y[3][0])) : k0; \
      k1 = take ? (__uint_as_float(Y[0][2]) + __uint_as_float(Y[1][2])) + (__uint_as_float(Y[2][2]) + __uint_as_float(Y[3][2])) : k1; \
      g0 = take && hh < NFW ? __uint_as_float(Y[4][0]) : g0; g1 = take && hh < NFW ? __uint_as_float(Y[4][2]) : g1; \
      done = done || take;                                                                           \
      vy = done ? WS_END : vy; vf = done ? WS_END : vf; vx = done ? WS_END : vx;                     \
      a0 = t32_sum(k0 + dpp<XOR8>(k0), g0 + dpp<XOR8>(g0));        \
      a0 = t16_sum(a0, t32_sum(k1 + dpp<XOR8>(k1), g1 + dpp<XOR8>(g1)));     \
      const unsigned long long miss = __builtin_amdgcn_ballot_w64(!done);                            \
      if (miss == 0ull) break;                                                                       \
      if (__builtin_popcountll(miss) > POLL_MANY) __builtin_amdgcn_s_sleep(POLL_SLEEP);              \
      asm volatile("" ::: "memory"); }
    for (unsigned spin = 0; spin < SPIN_CAP; ++spin) { SWEEP(ya) CHECK(ya) }
#undef SWEEP
#undef CHECK
    if (c < 8) lds[L_RED + w * 32 + (r >> 1) * 16 + 2 * c + (r & 1)] = __float_as_uint(a0);
    LGKM0();
    unsigned tk = 0;
    if (lane == 0) tk = __hip_atomic_fetch_add((LAS unsigned*)(lds + L_CNT), 16u, __ATOMIC_RELAXED, __HIP_MEMORY_SCOPE_WORKGROUP);
    if (__builtin_amdgcn_readfirstlane(tk) >= 48u) {
      asm volatile("" ::: "memory");
      if (lane < 8) {
        const float* red = (const float*)lds + L_RED + 2 * lane;
        const f32x2 A = (*(const f32x2*)(red) + *(const f32x2*)(red + 32)) + (*(const f32x2*)(red + 64) + *(const f32x2*)(red + 96));
        const f32x2 F = (*(const f32x2*)(red + 16) + *(const f32x2*)(red + 48)) + (*(const f32x2*)(red + 80) + *(const f32x2*)(red + 112));
        *(unsigned*)(out + cb * DM + cg * 16 + 2 * lane) = pack2(rbf(blo(xv) + rbf(A[0])) + rbf(F[0]), rbf(bhi(xv) + rbf(A[1])) + rbf(F[1]));
      }
      if (ci == 0 && lane == 0) __builtin_amdgcn_raw_buffer_store_b32(tag, rw, WS_E, 0, 16);
    }
  }
  STAMP(7);
  STAMP_FLUSH;
}

}

extern "C" void kernel_launch(void* const* d_in, const int* in_sizes, int n_in, void* d_out, int out_size, void* d_ws,
                              size_t ws_size, hipStream_t stream) {
  attn_kernel<<<NWG, NTHR, 0, stream>>>((const u16*)d_in[1], (const u16*)d_in[2], (const u16*)d_in[0], (const u16*)d_in[3], (unsigned*)d_ws,
                                        (u16*)d_out, (const u16*)d_in[4], (const u16*)d_in[8], (const u16*)d_in[6], (const u16*)d_in[7]);
}
```

```cpp
#include <hip/hip_runtime.h>
#include <hip/hip_bf16.h>
#include <cstdint>

namespace {

constexpr int NB = 2, DM = 512, NH = 32, SK = 16384, DH = 128, FF = 1792;
constexpr int NSPLIT = 4;
constexpr int NTHR = 512;
constexpr int FSH = 2;
constexpr int NXR = 2;
constexpr int WQ_INFL = 8;
constexpr int NWG = NB * NH * NSPLIT;
constexpr int STEPK = 64, STEPB = STEPK * DH * 2;
constexpr int NSTEP = SK / NSPLIT / STEPK;
constexpr int NREVMAX = (NSTEP + FSH + 3) / 4;
constexpr int FSL = 64, NFW = FF / FSL;

constexpr int WS_E = 0;
constexpr int WS_ML = 4096;
constexpr int WS_F = WS_ML + NB * NH * 256;
constexpr int WS_Y = WS_F + NB * 32 * NFW * 128;
constexpr int WS_END = WS_Y + NB * 32 * 128 * 128;
constexpr unsigned SPIN_CAP = 1u << 20;

typedef unsigned u32x4 __attribute__((ext_vector_type(4)));
typedef float f32x4 __attribute__((ext_vector_type(4)));
typedef float f32x2 __attribute__((ext_vector_type(2)));
typedef __bf16 bf16x2 __attribute__((ext_vector_type(2)));
typedef __bf16 bf16x8 __attribute__((ext_vector_type(8)));
typedef unsigned short u16;
#define GAS __attribute__((address_space(1)))
#define LAS __attribute__((address_space(3)))

__device__ __forceinline__ float dot2(unsigned a, unsigned b, float acc) {
  return __builtin_amdgcn_fdot2_f32_bf16(__builtin_bit_cast(bf16x2, a), __builtin_bit_cast(bf16x2, b), acc, false);
}
__device__ __forceinline__ float blo(unsigned u) { return __builtin_bit_cast(float, u << 16); }
__device__ __forceinline__ float bhi(unsigned u) { return __builtin_bit_cast(float, u & 0xffff0000u); }
__device__ __forceinline__ float rbf(float f) { return (float)(__bf16)f; }
__device__ __forceinline__ unsigned pack2(float lo, float hi) {
  bf16x2 v; v.x = (__bf16)lo; v.y = (__bf16)hi; return __builtin_bit_cast(unsigned, v);
}
template <int CTRL> __device__ __forceinline__ float dpp(float x) {
  return __builtin_bit_cast(float, __builtin_amdgcn_mov_dpp(__builtin_bit_cast(int, x), CTRL, 0xf, 0xf, true));
}
constexpr int XOR1 = 0xB1, XOR2 = 0x4E, XOR7 = 0x141, XOR8 = 0x128, BCAST = 0x150;
__device__ __forceinline__ float row16_sum(float x) {
  x += dpp<XOR1>(x); x += dpp<XOR2>(x); x += dpp<XOR7>(x); x += dpp<XOR8>(x); return x;
}
__device__ __forceinline__ float row16_max(float x) {
  x = fmaxf(x, dpp<XOR1>(x)); x = fmaxf(x, dpp<XOR2>(x)); x = fmaxf(x, dpp<XOR7>(x)); return fmaxf(x, dpp<XOR8>(x));
}
__device__ __forceinline__ float t16_sum(float a, float b) {
  auto s = __builtin_amdgcn_permlane16_swap(__float_as_uint(a), __float_as_uint(b), false, false);
  return __uint_as_float(s[0]) + __uint_as_float(s[1]);
}
__device__ __forceinline__ float t32_sum(float a, float b) {
  auto s = __builtin_amdgcn_permlane32_swap(__float_as_uint(a), __float_as_uint(b), false, false);
  return __uint_as_float(s[0]) + __uint_as_float(s[1]);
}
__device__ __forceinline__ float xrow_sum(float x) { x = t16_sum(x, x); return t32_sum(x, x); }
__device__ __forceinline__ float xrow_max(float x) {
  auto s = __builtin_amdgcn_permlane16_swap(__float_as_uint(x), __float_as_uint(x), false, false);
  x = fmaxf(__uint_as_float(s[0]), __uint_as_float(s[1]));
  auto t = __builtin_amdgcn_permlane32_swap(__float_as_uint(x), __float_as_uint(x), false, false);
  return fmaxf(__uint_as_float(t[0]), __uint_as_float(t[1]));
}
__device__ __forceinline__ float wave_sum(float x) { return xrow_sum(row16_sum(x)); }
typedef __amdgpu_buffer_rsrc_t rsrc_t;
__device__ __forceinline__ rsrc_t mkrsrc(const void* p, int bytes) { return __builtin_amdgcn_make_buffer_rsrc((void*)p, 0, bytes, 0x00020000); }
__device__ __forceinline__ u32x4 bld(rsrc_t r, int voff, int soff) { return __builtin_amdgcn_raw_buffer_load_b128(r, voff, soff, 2); }
__device__ __forceinline__ u32x4 bldc(rsrc_t r, int voff, int soff) { return __builtin_amdgcn_raw_buffer_load_b128(r, voff, soff, 0); }
__device__ __forceinline__ u32x4 gld(rsrc_t r, int voff, int soff) { return __builtin_amdgcn_raw_buffer_load_b128(r, voff, soff, 16); }
__device__ __forceinline__ void gst(rsrc_t r, int voff, int soff, float v0, float v1, unsigned tag) {
  __builtin_amdgcn_raw_buffer_store_b128(u32x4{__float_as_uint(v0), tag, __float_as_uint(v1), tag}, r, voff, soff, 16);
}
#define SB() __builtin_amdgcn_sched_barrier(0)
__device__ __forceinline__ unsigned load_tag(const unsigned* ws) {
  unsigned e;
  asm volatile("s_load_dword %0, %1, 0x0\n\ts_waitcnt lgkmcnt(0)" : "=s"(e) : "s"(ws) : "memory");
  return e + 1u;
}
#define PIN4(a) asm volatile("" : "+v"((a)[0]), "+v"((a)[1]), "+v"((a)[2]), "+v"((a)[3])::"memory"); SB()
#define LGKM0() asm volatile("s_waitcnt lgkmcnt(0)" ::: "memory")
#define CFENCE() asm volatile("" ::: "memory")
#define BARRIER() do { LGKM0(); __builtin_amdgcn_s_barrier(); CFENCE(); } while (0)
__device__ __forceinline__ f32x4 mfma(u32x4 a, u32x4 b, f32x4 c) {
  return __builtin_amdgcn_mfma_f32_16x16x32_bf16(__builtin_bit_cast(bf16x8, a), __builtin_bit_cast(bf16x8, b), c, 0, 0, 0);
}
__device__ __forceinline__ u32x4 bfrag(const u32x4* w, int e) {
  u32x4 o;
#pragma unroll
  for (int jj = 0; jj < 4; ++jj) o[jj] = __builtin_amdgcn_perm(w[2 * jj + 1][e >> 1], w[2 * jj][e >> 1], (e & 1) ? 0x07060302u : 0x05040100u);
  return o;
}
__device__ __forceinline__ u32x4 layernorm8(u32x4 xa) {
  float e[8];
#pragma unroll
  for (int k = 0; k < 4; ++k) { e[2 * k] = blo(xa[k]); e[2 * k + 1] = bhi(xa[k]); }
  const float mean = wave_sum(((e[0] + e[1]) + (e[2] + e[3])) + ((e[4] + e[5]) + (e[6] + e[7]))) * (1.0f / DM);
  float q = 0.f;
#pragma unroll
  for (int k = 0; k < 8; ++k) { const float d = e[k] - mean; q = fmaf(d, d, q); }
  const float var = wave_sum(q) * (1.0f / DM);
  const float mb = rbf(mean), sd = rbf(sqrtf(rbf(rbf(var) + 1e-5f)));
  const float ri = 1.0f / sd;
  u32x4 o;
#pragma unroll
  for (int k = 0; k < 4; ++k) {
    const float d0 = rbf(e[2 * k] - mb), d1 = rbf(e[2 * k + 1] - mb);
    float q0 = d0 * ri, q1 = d1 * ri;
    q0 = fmaf(fmaf(-q0, sd, d0), ri, q0); q1 = fmaf(fmaf(-q1, sd, d1), ri, q1);
    o[k] = pack2(q0, q1);
  }
  return o;
}

constexpr int L_QP = 0;
constexpr int L_A = L_QP + 4 * DH;
constexpr int L_CNT = L_A + 1024, L_TAG = L_CNT + 1, L_MID = L_CNT + 2, L_FW = L_CNT + 4;
constexpr int L_RED = L_CNT + 16;
constexpr int L_XN = L_RED + 128;
constexpr int L_GU = L_XN + 4 * 128;
constexpr int L_H = L_GU + 4 * 256;
constexpr int L_S = L_H + 4 * 64;
constexpr int L_TOT = L_S + 4 * NREVMAX * 64;

__global__ void __launch_bounds__(NTHR, 2) attn_kernel(const u16* __restrict__ kc, const u16* __restrict__ vc, const u16* __restrict__ x,
                                                       const u16* __restrict__ wq, unsigned* ws, u16* __restrict__ out,
                                                       const u16* __restrict__ wo, const u16* __restrict__ w2,
                                                       const u16* __restrict__ w1, const u16* __restrict__ w3) {
  __shared__ __attribute__((aligned(16))) unsigned lds[L_TOT];
  const int bid = blockIdx.x;
  const int tid = threadIdx.x, lane = tid & 63, wv = __builtin_amdgcn_readfirstlane(tid >> 6);
  const int j = bid >> 3, h = (bid & 7) * 4 + (j >> 3), b = (j >> 2) & 1, s = j & 3;
  const int r = lane >> 4, c = lane & 15;
  const bool fgrp = (bid & 1) == 0 && (j >> 2) < 7;
  const int w = wv - 4;
  const int fs = fgrp ? FSH : 0;
  int nst = NSTEP + (s == 0 ? -3 * fs : fs);
  int kvb = ((b * NH + h) * NSPLIT * NSTEP + s * NSTEP + (s == 0 ? 0 : (s - 4) * fs)) * STEPB;
  int so0 = w * 4096 + ((2 * s + b) * 8 + (s == 0 ? 0 : (4 - s) * fs)) * STEPB;
  asm volatile("" : "+s"(nst), "+s"(kvb), "+s"(so0) : "s"(kc), "s"(vc), "s"(x), "s"(wq), "s"(ws), "s"(out));

  if (wv < 4) {
    const int a = wv;
    __builtin_amdgcn_s_setprio(3);
    const rsrc_t rx = mkrsrc(x, NB * DM * 2);
    const u32x4 xa = bldc(rx, lane * 16, b * (DM * 2));
    u32x4 wr[32];
    const rsrc_t rq = mkrsrc(wq + h * DH, (DM * NH * DH - h * DH) * 2);
    const int vq = r * (8 * NH * DH * 2) + c * 16;
#define LD_WQ(i) { if ((i) >= WQ_INFL) asm volatile("s_waitcnt vmcnt(%0)" ::"n"(WQ_INFL - 1) : "memory"); \
      wr[i] = bldc(rq, vq, (128 * a + 32 * ((((i) >> 3) + s) & 3) + ((i) & 7)) * (NH * DH * 2)); SB(); }
#pragma unroll
    for (int i = 0; i < WQ_INFL; ++i) LD_WQ(i)
    if (a == 0) {
      const unsigned t = load_tag(ws);
      if (lane == 0) { lds[L_CNT] = 0u; lds[L_TAG] = t; lds[L_MID] = 0u; }
    }
    SB();
    unsigned* xn = lds + L_XN + a * 128;
    {
      const u32x4 n0 = layernorm8(xa);
      if (r == a) *(u32x4*)(xn + 64 * b + 4 * c) = n0;
    }
    CFENCE(); SB();
    f32x4 d[8];
#pragma unroll
    for (int e = 0; e < 8; ++e) d[e] = f32x4{0.f, 0.f, 0.f, 0.f};
#pragma unroll
    for (int ks = 0; ks < 4; ++ks) {
#pragma unroll
      for (int i = 8 * ks + WQ_INFL; i < 8 * ks + 8 + WQ_INFL; ++i) if (i < 32) LD_WQ(i)
      const u32x4 af = *(const u32x4*)(xn + 64 * b + 16 * ((ks + s) & 3) + 4 * r);
#pragma unroll
      for (int e = 0; e < 8; ++e) d[e] = mfma(af, bfrag(wr + 8 * ks, e), d[e]);
      SB();
    }
#undef LD_WQ
    if (r == 0) {
      *(f32x4*)(lds + L_QP + a * DH + 8 * c) = f32x4{d[0][0], d[1][0], d[2][0], d[3][0]};
      *(f32x4*)(lds + L_QP + a * DH + 8 * c + 4) = f32x4{d[4][0], d[5][0], d[6][0], d[7][0]};
    }
    BARRIER();
    const unsigned tag = lds[L_TAG];
    const rsrc_t rw = mkrsrc(ws, WS_END);
    u32x4 bf[32];
    {
      const rsrc_t ro = mkrsrc(wo + (size_t)h * DH * DM, DH * DM * 2);
#pragma unroll
      for (int ks = 0; ks < 4; ++ks) {
#pragma unroll
        for (int i = 0; i < 8; ++i) {
          __builtin_amdgcn_s_sleep(24);
          wr[i] = bld(ro, r * 8192 + c * 16 + (i & 3) * 1024, ks * 32768 + (i >> 2) * 4096 + a * 256);
          SB();
        }
#pragma unroll
        for (int e = 0; e < 8; ++e) { bf[8 * ks + e] = bfrag(wr, e); asm volatile("" : "+v"(bf[8 * ks + e])); }
        SB();
      }
    }
    if (fgrp && s == 0) {
      const int sl = ((bid & 7) >> 1) * 7 + (j >> 2), f0 = sl * FSL;
      const u32x4 xo = bldc(rx, lane * 16, (b ^ 1) * (DM * 2));
      const GAS u32x4* p13 = (const GAS u32x4*)((c < 8 ? w1 : w3) + (size_t)(128 * a + 8 * r) * FF + f0 + 8 * (c & 7));
#pragma unroll
      for (int i = 0; i < 8; ++i) wr[i] = __builtin_nontemporal_load(p13 + i * (FF / 8));
      SB();
      {
        const u32x4 n1 = layernorm8(xo);
        if (r == a) *(u32x4*)(xn + 64 * (b ^ 1) + 4 * c) = n1;
      }
      CFENCE();
#pragma unroll
      for (int e = 0; e < 8; ++e) d[e] = f32x4{0.f, 0.f, 0.f, 0.f};
      const rsrc_t r2 = mkrsrc(w2 + (size_t)f0 * DM, FSL * DM * 2);
#pragma unroll
      for (int ks = 0; ks < 4; ++ks) {
        const u32x4 af = *(const u32x4*)(xn + 64 * (c & 1) + 16 * ks + 4 * r);
#pragma unroll
        for (int e = 0; e < 8; ++e) d[e] = mfma(af, bfrag(wr, e), d[e]);
        SB();
#pragma unroll
        for (int jj = 0; jj < 8; ++jj)
          wr[jj] = ks < 3 ? __builtin_nontemporal_load(p13 + (32 * (ks + 1) + jj) * (FF / 8))
                          : bld(r2, r * 8192 + c * 16 + (jj & 3) * 1024, (jj >> 2) * 4096 + a * 256);
        SB();
      }
      if (r < 2) {
        float* gu = (float*)lds + L_GU + a * 256 + r * 128 + 8 * c;
        *(f32x4*)(gu) = r ? f32x4{d[0][1], d[1][1], d[2][1], d[3][1]} : f32x4{d[0][0], d[1][0], d[2][0], d[3][0]};
        *(f32x4*)(gu + 4) = r ? f32x4{d[4][1], d[5][1], d[6][1], d[7][1]} : f32x4{d[4][0], d[5][0], d[6][0], d[7][0]};
      }
      LGKM0();
      if (lane == 0) __hip_atomic_fetch_add((LAS unsigned*)(lds + L_CNT), 1u, __ATOMIC_RELAXED, __HIP_MEMORY_SCOPE_WORKGROUP);
      while (*(volatile LAS unsigned*)(lds + L_CNT) < 4u) __builtin_amdgcn_s_sleep(1);
      CFENCE();
      {
        const float* gu = (const float*)lds + L_GU + (lane >> 5) * 128 + 2 * (lane & 31);
        f32x2 g = {0.f, 0.f}, u = {0.f, 0.f};
#pragma unroll
        for (int aa = 0; aa < 4; ++aa) { g += *(const f32x2*)(gu + aa * 256); u += *(const f32x2*)(gu + aa * 256 + 64); }
        float hv[2];
#pragma unroll
        for (int k = 0; k < 2; ++k) { const float gg = rbf(g[k]); hv[k] = rbf(gg / (1.0f + __expf(-gg))) * rbf(u[k]); }
        lds[L_H + a * 64 + lane] = pack2(hv[0], hv[1]);
      }
      CFENCE();
#pragma unroll
      for (int e = 0; e < 8; ++e) d[e] = f32x4{0.f, 0.f, 0.f, 0.f};
#pragma unroll
      for (int ks = 0; ks < 2; ++ks) {
        const u32x4 af = *(const u32x4*)(lds + L_H + a * 64 + 32 * (c & 1) + 16 * ks + 4 * r);
#pragma unroll
        for (int e = 0; e < 8; ++e) d[e] = mfma(af, bfrag(wr, e), d[e]);
        SB();
        if (ks == 0) {
#pragma unroll
          for (int jj = 0; jj < 8; ++jj) wr[jj] = bld(r2, r * 8192 + c * 16 + (jj & 3) * 1024, 32768 + (jj >> 2) * 4096 + a * 256);
          SB();
        }
      }
      {
        const int fo = WS_F + (((r & 1) * 32 + 8 * a + (c >> 1)) * NFW + sl) * 128 + (c & 1) * 64 + (r >> 1) * 32;
        float t[4];
#pragma unroll
        for (int e = 0; e < 4; ++e) {
          const float p0 = (r & 1) ? d[e][1] : d[e][0], p1 = (r & 1) ? d[4 + e][1] : d[4 + e][0];
          t[e] = r < 2 ? p0 : p1;
        }
        gst(rw, fo, 0, t[0], t[1], tag); gst(rw, fo + 16, 0, t[2], t[3], tag);
      }
      SB();
    }
    if (a == 0) {
      while (*(volatile LAS unsigned*)(lds + L_MID) < 4u) __builtin_amdgcn_s_sleep(16);
      if (!(fgrp && s == 0)) for (int i = 0; i < 3; ++i) __builtin_amdgcn_s_sleep(127);
      u32x4 ml;
      for (unsigned spin = 0; spin < SPIN_CAP; ++spin) {
        CFENCE();
        ml = gld(rw, c * 16, WS_ML + (b * NH + h) * 256);
        if (__builtin_amdgcn_ballot_w64(((ml[1] ^ tag) | (ml[3] ^ tag)) != 0u) == 0ull) break;
        __builtin_amdgcn_s_sleep(64);
      }
      const float mw = __uint_as_float(ml[0]), M = row16_max(mw);
      const float e = __builtin_amdgcn_exp2f(mw - M);
      const float f = e / row16_sum(e * __uint_as_float(ml[2]));
      if ((lane >> 2) == s) lds[L_FW + (lane & 3)] = __float_as_uint(f);
    }
    BARRIER();
    u32x4 af[4];
#pragma unroll
    for (int ks = 0; ks < 4; ++ks) af[ks] = *(const u32x4*)(lds + L_A + (ks * 64 + r * 16 + (c & 7)) * 4);
    const f32x2 fw = *(const f32x2*)(lds + L_FW + 2 * (r & 1));
    const float f0 = fw[0], f1 = fw[1];
#pragma unroll
    for (int e = 0; e < 8; ++e) d[e] = f32x4{0.f, 0.f, 0.f, 0.f};
#pragma unroll
    for (int ks = 0; ks < 4; ++ks)
#pragma unroll
      for (int e = 0; e < 8; ++e) d[e] = mfma(af[ks], bf[8 * ks + e], d[e]);
    float v[4];
#pragma unroll
    for (int e = 0; e < 4; ++e) {
      const float p0 = d[e][0] + d[e][1], p1 = d[e][2] + d[e][3], q0 = d[4 + e][0] + d[4 + e][1], q1 = d[4 + e][2] + d[4 + e][3];
      v[e] = f0 * (r < 2 ? p0 : q0) + f1 * (r < 2 ? p1 : q1);
    }
    gst(rw, (c >> 1) * (128 * 128) + (c & 1) * 64 + r * 16, WS_Y + ((b * 32 + 8 * a) * 128 + h * NSPLIT + s) * 128,
        t16_sum(v[0], v[2]), t16_sum(v[1], v[3]), tag);
    return;
  }

  u32x4 ring[16 * (1 + NXR)];
  const int vo = lane * 16;
  const int lim = nst * STEPB;
  const rsrc_t rk = mkrsrc((const char*)kc + (unsigned)kvb, lim), rv = mkrsrc((const char*)vc + (unsigned)kvb, lim);
  int so = so0;
#define ADV() so += STEPB; so = so >= lim ? so - lim : so;
#define FILL(rs, g, o) _Pragma("unroll") for (int i = 0; i < 4; ++i) { ring[(g) * 4 + i] = bld(rs, vo + i * 1024, o); SB(); }
#pragma unroll
  for (int g = 4; g < 4 + 4 * NXR; ++g) { FILL(rk, g, so) ADV() }
#pragma unroll
  for (int g = 0; g < 4; ++g) { FILL(rk, g, so) ADV() }
  BARRIER();
  unsigned qp[4];
  {
    f32x4 t0 = *(const f32x4*)(lds + L_QP + 8 * c), t1 = *(const f32x4*)(lds + L_QP + 8 * c + 4);
#pragma unroll
    for (int aa = 1; aa < 4; ++aa) {
      t0 += *(const f32x4*)(lds + L_QP + aa * DH + 8 * c);
      t1 += *(const f32x4*)(lds + L_QP + aa * DH + 8 * c + 4);
    }
    qp[0] = pack2(t0[0], t0[1]); qp[1] = pack2(t0[2], t0[3]); qp[2] = pack2(t1[0], t1[1]); qp[3] = pack2(t1[2], t1[3]);
  }
  constexpr float SCALE = 0.088388347648318440f, LOG2E = 1.4426950408889634f;
  float* sl = (float*)lds + L_S + w * (NREVMAX * 64) + lane;
  float m = -INFINITY;
  float xs[16];
#define QK(g, slot)                                                                                  \
    _Pragma("unroll") for (int i = 0; i < 4; ++i) {                                                  \
      float d_ = 0.f;                                                                                \
      _Pragma("unroll") for (int k = 0; k < 4; ++k) d_ = dot2(ring[(slot) * 4 + i][k], qp[k], d_);   \
      xs[(g) * 4 + i] = d_;                                                                          \
    }                                                                                                \
    PIN4(xs + (g) * 4);
#define TR(HALF, CTRL) _Pragma("unroll") for (int k = 0; k < (HALF); ++k) {                          \
      const float t1_ = xs[k] + dpp<CTRL>(xs[k]), t2_ = xs[k + (HALF)] + dpp<CTRL>(xs[k + (HALF)]);  \
      xs[k] = (c & (HALF)) ? t2_ : t1_; }
#define SCORE(rev, nvalid)                                                                           \
  {                                                                                                  \
    TR(8, XOR8) TR(4, XOR7) TR(2, XOR2) TR(1, XOR1)                                                  \
    float t = rbf(rbf(xs[0]) * SCALE) * LOG2E;                     \
    t = (c >> 2) < (nvalid) ? t : -INFINITY;                                                         \
    m = fmaxf(m, t);                                                                                 \
    sl[(rev) * 64] = t;                                                                              \
    SB();                                                                                            \
  }
  const int nrev = (nst + 3) >> 2;
  {
    int left = nst - 4 * (1 + NXR);
    rsrc_t rn = rk;
#pragma nounroll
    for (int rev = NXR; rev < nrev; ++rev) {
      if (rev == nrev - 1) { rn = rv; so = so0; left = 4; }
#define STEP(g) { const int o_ = left > (g) ? so : lim; QK(g, g) FILL(rn, g, o_) ADV() }
      STEP(0) STEP(1) STEP(2) STEP(3)
#undef STEP
      SCORE(rev, nst - 4 * rev)
      left -= 4;
#define XREV(e) if constexpr ((e) < NXR) { if (rev == NXR + (e)) {                                   \
        _Pragma("unroll") for (int g = 0; g < 4; ++g) { PIN4(ring + (4 * (1 + (e)) + g) * 4); QK(g, 4 * (1 + (e)) + g) } \
        SCORE(e, 4) } }
      XREV(0) XREV(1)
#undef XREV
    }
  }
  {
    m = xrow_max(row16_max(m));
    float l = 0.f;
#pragma unroll
    for (int rev = 0; rev < NREVMAX; ++rev) {
      const float t = rev < NREVMAX - 3 || rev < nrev ? sl[rev * 64] : -INFINITY;
      const float p = __builtin_amdgcn_exp2f(t - m);
      l += p;
      sl[rev * 64] = p;
    }
    l = wave_sum(l);
    gst(mkrsrc(ws, WS_END), lane == 0 ? 0 : WS_END, WS_ML + (b * NH + h) * 256 + (4 * s + w) * 16, m, l, lds[L_TAG]);
    if (lane == 0) __hip_atomic_fetch_add((LAS unsigned*)(lds + L_MID), 1u, __ATOMIC_RELAXED, __HIP_MEMORY_SCOPE_WORKGROUP);
    SB();
  }
  f32x2 acc[4];
#pragma unroll
  for (int k = 0; k < 4; ++k) acc[k] = f32x2{0.f, 0.f};
#define PV1(g, i) { const float pb = dpp<BCAST + 4 * (g) + (i)>(p); const u32x4 vv = ring[(g) * 4 + (i)];  \
      _Pragma("unroll") for (int k = 0; k < 4; ++k)                                                  \
        acc[k] = __builtin_elementwise_fma(f32x2{pb, pb}, f32x2{blo(vv[k]), bhi(vv[k])}, acc[k]); }
#define PV(g) PV1(g, 0) PV1(g, 1) PV1(g, 2) PV1(g, 3) PIN4(acc);
  int left = nst - 4;
  float pn = sl[0];
#pragma nounroll
  for (int rev = 1; rev < nrev; ++rev) {
    const float p = pn;
    pn = sl[rev * 64];
#define STEP(g) { const int o_ = left > (g) ? so : lim; PV(g) FILL(rv, g, o_) ADV() }
    STEP(0) STEP(1) STEP(2) STEP(3)
#undef STEP
    left -= 4;
  }
  {
    const float p = pn;
    PV(0)
    if (left > -3) { PV(1) }
    if (left > -2) { PV(2) }
    if (left > -1) { PV(3) }
  }
  {
    const float a0 = t32_sum(acc[0][0], acc[2][0]), a1 = t32_sum(acc[0][1], acc[2][1]);
    const float a2 = t32_sum(acc[1][0], acc[3][0]), a3 = t32_sum(acc[1][1], acc[3][1]);
    const float t0 = t16_sum(a0, a2), t1 = t16_sum(a1, a3);
    const unsigned hi = pack2(t0, t1), lo = pack2(t0 - blo(hi), t1 - bhi(hi));
    unsigned* pa = lds + L_A + ((c >> 2) * 64 + (c & 3) * 16 + 2 * w) * 4 + r;
    pa[0] = hi; pa[4] = lo;
  }
  BARRIER();
  if ((bid & 1) == 0 && j < 16) {
    const int ci = ((bid & 7) >> 1) * 16 + j, cb = ci >> 5, cg = ci & 31;
    const int t = tid - 256, pc = t & 7, hh = t >> 3;
    const unsigned tag = lds[L_TAG];
    const rsrc_t rw = mkrsrc(ws, WS_END);
    const int sy = WS_Y + (cb * 32 + cg) * (128 * 128);
    int vy = hh * 512 + pc * 16;
    int vf = hh < NFW ? WS_F + ((cb * 32 + cg) * NFW + hh) * 128 + pc * 16 : sy + vy;
    int vx = ci == 0 ? WS_ML + NH * 256 + (t & 127) * 64 : vf;
    const unsigned xv = *(const unsigned*)(x + cb * DM + cg * 16 + 2 * pc);
    float k0 = 0.f, k1 = 0.f, g0 = 0.f, g1 = 0.f, a0 = 0.f;
    bool done = false;
    for (unsigned spin = 0; spin < SPIN_CAP; ++spin) {
      CFENCE();
      u32x4 yy[5];
#pragma unroll
      for (int i = 0; i < 4; ++i) yy[i] = gld(rw, vy + i * 128, sy);
      yy[4] = gld(rw, vf, 0);
      unsigned bad = __builtin_amdgcn_raw_buffer_load_b32(rw, vx, 4, 16) ^ tag;
#pragma unroll
      for (int i = 0; i < 5; ++i) bad |= (yy[i][1] ^ tag) | (yy[i][3] ^ tag);
      const bool take = !done && bad == 0u;
      k0 = take ? (__uint_as_float(yy[0][0]) + __uint_as_float(yy[1][0])) + (__uint_as_float(yy[2][0]) + __uint_as_float(yy[3][0])) : k0;
      k1 = take ? (__uint_as_float(yy[0][2]) + __uint_as_float(yy[1][2])) + (__uint_as_float(yy[2][2]) + __uint_as_float(yy[3][2])) : k1;
      g0 = take && hh < NFW ? __uint_as_float(yy[4][0]) : g0; g1 = take && hh < NFW ? __uint_as_float(yy[4][2]) : g1;
      done = done || take;
      vy = done ? WS_END : vy; vf = done ? WS_END : vf; vx = done ? WS_END : vx;
      a0 = t32_sum(k0 + dpp<XOR8>(k0), g0 + dpp<XOR8>(g0));
      a0 = t16_sum(a0, t32_sum(k1 + dpp<XOR8>(k1), g1 + dpp<XOR8>(g1)));
      const unsigned long long miss = __builtin_amdgcn_ballot_w64(!done);
      if (miss == 0ull) break;
      if (__builtin_popcountll(miss) > 32) __builtin_amdgcn_s_sleep(16);
    }
    if (c < 8) lds[L_RED + w * 32 + (r >> 1) * 16 + 2 * c + (r & 1)] = __float_as_uint(a0);
    LGKM0();
    unsigned tk = 0;
    if (lane == 0) tk = __hip_atomic_fetch_add((LAS unsigned*)(lds + L_CNT), 16u, __ATOMIC_RELAXED, __HIP_MEMORY_SCOPE_WORKGROUP);
    if (__builtin_amdgcn_readfirstlane(tk) >= 48u) {
      CFENCE();
      if (lane < 8) {
        const float* red = (const float*)lds + L_RED + 2 * lane;
        const f32x2 A = (*(const f32x2*)(red) + *(const f32x2*)(red + 32)) + (*(const f32x2*)(red + 64) + *(const f32x2*)(red + 96));
        const f32x2 F = (*(const f32x2*)(red + 16) + *(const f32x2*)(red + 48)) + (*(const f32x2*)(red + 80) + *(const f32x2*)(red + 112));
        *(unsigned*)(out + cb * DM + cg * 16 + 2 * lane) = pack2(rbf(blo(xv) + rbf(A[0])) + rbf(F[0]), rbf(bhi(xv) + rbf(A[1])) + rbf(F[1]));
      }
      if (ci == 0 && lane == 0) __builtin_amdgcn_raw_buffer_store_b32(tag, rw, WS_E, 0, 16);
    }
  }
}

}

extern "C" void kernel_launch(void* const* d_in, const int* in_sizes, int n_in, void* d_out, int out_size, void* d_ws,
                              size_t ws_size, hipStream_t stream) {
  attn_kernel<<<NWG, NTHR, 0, stream>>>((const u16*)d_in[1], (const u16*)d_in[2], (const u16*)d_in[0], (const u16*)d_in[3], (unsigned*)d_ws,
                                        (u16*)d_out, (const u16*)d_in[4], (const u16*)d_in[8], (const u16*)d_in[6], (const u16*)d_in[7]);
}
```

```cpp
#include <hip/hip_runtime.h>
#include <hip/hip_bf16.h>
#include <cstdint>

#define WQ_INFL 8
#define WO_SLEEP 16
#define CONS_PAR 0
#define POLL_MANY 64
#define POLL_SLEEP 16
#define OAUX 2
#define PHASE_FIX 1
#define ABL 0
#define ML_SLEEP 3
#define PIPE 1
#define SMUL 1
#define T1 3900
#define T2 7550
#define PSL 1
#define PGR 1
#define FFN_AHEAD 500
#define FFN_AHEAD2 150
#define CONS_AHEAD 100
#define EVEN_AHEAD 0
#define PB 0
#define LEAD 0
#define SPRINT 0
#define FSH 2
#define NXR 2

namespace {

constexpr int NB = 2, DM = 512, NH = 32, SK = 16384, DH = 128, FF = 1792;
constexpr int NSPLIT = 4;
constexpr int NTHR = 512;
constexpr int NWG = NB * NH * NSPLIT;
constexpr int STEPK = 64, STEPB = STEPK * DH * 2;
constexpr int NSTEP = SK / NSPLIT / STEPK;
constexpr int NREVMAX = (NSTEP + FSH + 3) / 4;
constexpr int FSL = 64, NFW = FF / FSL;

constexpr int WS_E = 0;
constexpr int WS_ML = 4096;
constexpr int WS_F = WS_ML + NB * NH * 256;
constexpr int WS_Y = WS_F + NB * 32 * NFW * 128;
constexpr int WS_END = WS_Y + NB * 32 * 128 * 128;
constexpr unsigned SPIN_CAP = 1u << 20;

typedef unsigned u32x4 __attribute__((ext_vector_type(4)));
typedef float f32x4 __attribute__((ext_vector_type(4)));
typedef float f32x2 __attribute__((ext_vector_type(2)));
typedef __bf16 bf16x2 __attribute__((ext_vector_type(2)));
typedef __bf16 bf16x8 __attribute__((ext_vector_type(8)));
typedef unsigned short u16;
#define GAS __attribute__((address_space(1)))
#define LAS __attribute__((address_space(3)))

__device__ __forceinline__ float dot2(unsigned a, unsigned b, float acc) {
  return __builtin_amdgcn_fdot2_f32_bf16(__builtin_bit_cast(bf16x2, a), __builtin_bit_cast(bf16x2, b), acc, false);
}
__device__ __forceinline__ float blo(unsigned u) { return __builtin_bit_cast(float, u << 16); }
__device__ __forceinline__ float bhi(unsigned u) { return __builtin_bit_cast(float, u & 0xffff0000u); }
__device__ __forceinline__ float rbf(float f) { return (float)(__bf16)f; }
__device__ __forceinline__ unsigned pack2(float lo, float hi) {
  bf16x2 v; v.x = (__bf16)lo; v.y = (__bf16)hi; return __builtin_bit_cast(unsigned, v);
}
template <int CTRL> __device__ __forceinline__ float dpp(float x) {
  return __builtin_bit_cast(float, __builtin_amdgcn_mov_dpp(__builtin_bit_cast(int, x), CTRL, 0xf, 0xf, true));
}
constexpr int XOR1 = 0xB1, XOR2 = 0x4E, XOR7 = 0x141, XOR8 = 0x128, BCAST = 0x150;
__device__ __forceinline__ float row16_sum(float x) {
  x += dpp<XOR1>(x); x += dpp<XOR2>(x); x += dpp<XOR7>(x); x += dpp<XOR8>(x); return x;
}
__device__ __forceinline__ float row16_max(float x) {
  x = fmaxf(x, dpp<XOR1>(x)); x = fmaxf(x, dpp<XOR2>(x)); x = fmaxf(x, dpp<XOR7>(x)); return fmaxf(x, dpp<XOR8>(x));
}
__device__ __forceinline__ float t16_sum(float a, float b) {
  auto s = __builtin_amdgcn_permlane16_swap(__float_as_uint(a), __float_as_uint(b), false, false);
  return __uint_as_float(s[0]) + __uint_as_float(s[1]);
}
__device__ __forceinline__ float t32_sum(float a, float b) {
  auto s = __builtin_amdgcn_permlane32_swap(__float_as_uint(a), __float_as_uint(b), false, false);
  return __uint_as_float(s[0]) + __uint_as_float(s[1]);
}
__device__ __forceinline__ float xrow_sum(float x) { x = t16_sum(x, x); return t32_sum(x, x); }
__device__ __forceinline__ float xrow_max(float x) {
  auto s = __builtin_amdgcn_permlane16_swap(__float_as_uint(x), __float_as_uint(x), false, false);
  x = fmaxf(__uint_as_float(s[0]), __uint_as_float(s[1]));
  auto t = __builtin_amdgcn_permlane32_swap(__float_as_uint(x), __float_as_uint(x), false, false);
  return fmaxf(__uint_as_float(t[0]), __uint_as_float(t[1]));
}
__device__ __forceinline__ float wave_sum(float x) { return xrow_sum(row16_sum(x)); }
typedef __amdgpu_buffer_rsrc_t rsrc_t;
__device__ __forceinline__ rsrc_t mkrsrc(const void* p, int bytes) { return __builtin_amdgcn_make_buffer_rsrc((void*)p, 0, bytes, 0x00020000); }
__device__ __forceinline__ u32x4 bld(rsrc_t r, int voff, int soff) { return __builtin_amdgcn_raw_buffer_load_b128(r, voff, soff, 2); }
__device__ __forceinline__ u32x4 bldc(rsrc_t r, int voff, int soff) { return __builtin_amdgcn_raw_buffer_load_b128(r, voff, soff, 0); }
__device__ __forceinline__ u32x4 gld(rsrc_t r, int voff, int soff) { return __builtin_amdgcn_raw_buffer_load_b128(r, voff, soff, 16); }
__device__ __forceinline__ void gst(rsrc_t r, int voff, int soff, float v0, float v1, unsigned tag) {
  __builtin_amdgcn_raw_buffer_store_b128(u32x4{__float_as_uint(v0), tag, __float_as_uint(v1), tag}, r, voff, soff, 16);
}
#define SB() __builtin_amdgcn_sched_barrier(0)
__device__ __forceinline__ unsigned load_tag(const unsigned* ws) {
  unsigned e;
  asm volatile("s_load_dword %0, %1, 0x0\n\ts_waitcnt lgkmcnt(0)" : "=s"(e) : "s"(ws) : "memory");
  return e + 1u;
}
#define PIN4(a) asm volatile("" : "+v"((a)[0]), "+v"((a)[1]), "+v"((a)[2]), "+v"((a)[3])::"memory"); SB()
#define LGKM0() asm volatile("s_waitcnt lgkmcnt(0)" ::: "memory")
__device__ __forceinline__ f32x4 mfma(u32x4 a, u32x4 b, f32x4 c) {
  return __builtin_amdgcn_mfma_f32_16x16x32_bf16(__builtin_bit_cast(bf16x8, a), __builtin_bit_cast(bf16x8, b), c, 0, 0, 0);
}
__device__ __forceinline__ u32x4 bfrag(const u32x4* w, int e) {
  u32x4 o;
#pragma unroll
  for (int jj = 0; jj < 4; ++jj) o[jj] = __builtin_amdgcn_perm(w[2 * jj + 1][e >> 1], w[2 * jj][e >> 1], (e & 1) ? 0x07060302u : 0x05040100u);
  return o;
}
__device__ __forceinline__ u32x4 layernorm8(u32x4 xa) {
  float e[8];
#pragma unroll
  for (int k = 0; k < 4; ++k) { e[2 * k] = blo(xa[k]); e[2 * k + 1] = bhi(xa[k]); }
  const float mean = wave_sum(((e[0] + e[1]) + (e[2] + e[3])) + ((e[4] + e[5]) + (e[6] + e[7]))) * (1.0f / DM);
  float q = 0.f;
#pragma unroll
  for (int k = 0; k < 8; ++k) { const float d = e[k] - mean; q = fmaf(d, d, q); }
  const float var = wave_sum(q) * (1.0f / DM);
  const float mb = rbf(mean), sd = rbf(sqrtf(rbf(rbf(var) + 1e-5f)));
  const float ri = 1.0f / sd;
  u32x4 o;
#pragma unroll
  for (int k = 0; k < 4; ++k) {
    const float d0 = rbf(e[2 * k] - mb), d1 = rbf(e[2 * k + 1] - mb);
    float q0 = d0 * ri, q1 = d1 * ri;
    q0 = fmaf(fmaf(-q0, sd, d0), ri, q0); q1 = fmaf(fmaf(-q1, sd, d1), ri, q1);
    o[k] = pack2(q0, q1);
  }
  return o;
}

#define STAMP(i)
#define STAMP_DECL
#define STAMP_FLUSH

constexpr int L_QP = 0;
constexpr int L_A = L_QP + 4 * DH;
constexpr int L_CNT = L_A + 1024, L_TAG = L_CNT + 1, L_MID = L_CNT + 2, L_FW = L_CNT + 4;
constexpr int L_RED = L_CNT + 16;
constexpr int L_XN = L_RED + 128;
constexpr int L_GU = L_XN + 4 * 128;
constexpr int L_H = L_GU + 4 * 256;
constexpr int L_S = L_H + 4 * 64;
constexpr int L_TOT = L_S + 4 * NREVMAX * 64;

__global__ void __launch_bounds__(NTHR, 2) attn_kernel(const u16* __restrict__ kc, const u16* __restrict__ vc, const u16* __restrict__ x,
                                                       const u16* __restrict__ wq, unsigned* ws, u16* __restrict__ out,
                                                       const u16* __restrict__ wo, const u16* __restrict__ w2,
                                                       const u16* __restrict__ w1, const u16* __restrict__ w3) {
  __shared__ __attribute__((aligned(16))) unsigned lds[L_TOT];
  STAMP_DECL; STAMP(0);
  const int bid = blockIdx.x;
  const int tid = threadIdx.x, lane = tid & 63, wv = __builtin_amdgcn_readfirstlane(tid >> 6);
  const int j = bid >> 3, h = (bid & 7) * 4 + (j >> 3), b = (j >> 2) & 1, s = j & 3;
  const int r = lane >> 4, c = lane & 15;
  const bool fgrp = !(ABL & 2) && (bid & 1) == 0 && (j >> 2) < 7;
  const int w = wv - 4;
  const int fs = fgrp ? FSH : 0;
  int nst = NSTEP + (s == 0 ? -3 * fs : fs);
  int kvb = ((b * NH + h) * NSPLIT * NSTEP + s * NSTEP + (s == 0 ? 0 : (s - 4) * fs)) * STEPB;
  int so0 = w * 4096 + ((2 * s + b) * 8 + (s == 0 ? 0 : (4 - s) * fs)) * STEPB;
  asm volatile("" : "+s"(nst), "+s"(kvb), "+s"(so0) : "s"(kc), "s"(vc), "s"(x), "s"(wq), "s"(ws), "s"(out));

  if (wv < 4) {
    const int a = wv;
    __builtin_amdgcn_s_setprio(3);
    const rsrc_t rx = mkrsrc(x, NB * DM * 2);
    const u32x4 xa = bldc(rx, lane * 16, b * (DM * 2));
    u32x4 wr[32];
    const rsrc_t rq = mkrsrc(wq + h * DH, (DM * NH * DH - h * DH) * 2);
    const int vq = r * (8 * NH * DH * 2) + c * 16;
#define LD_WQ(i) { if ((i) >= WQ_INFL) asm volatile("s_waitcnt vmcnt(%0)" ::"n"(WQ_INFL - 1) : "memory"); \
      wr[i] = (ABL & 8) ? xa : bldc(rq, vq, (128 * a + 32 * ((((i) >> 3) + s) & 3) + ((i) & 7)) * (NH * DH * 2)); SB(); }
#pragma unroll
    for (int i = 0; i < WQ_INFL; ++i) LD_WQ(i)
    STAMP(1);
    if (a == 0) {
      const unsigned t = load_tag(ws);
      if (lane == 0) { lds[L_CNT] = 0u; lds[L_TAG] = t; lds[L_MID] = 0u; }
    }
    SB();
    unsigned* xn = lds + L_XN + a * 128;
    {
      const u32x4 n0 = layernorm8(xa);
      if (r == a) *(u32x4*)(xn + 64 * b + 4 * c) = n0;
    }
    SB();
    STAMP(7);
    f32x4 d[8];
#pragma unroll
    for (int e = 0; e < 8; ++e) d[e] = f32x4{0.f, 0.f, 0.f, 0.f};
#pragma unroll
    for (int ks = 0; ks < 4; ++ks) {
#pragma unroll
      for (int i = 8 * ks + WQ_INFL; i < 8 * ks + 8 + WQ_INFL; ++i) if (i < 32) LD_WQ(i)
      const u32x4 af = *(const u32x4*)(xn + 64 * b + 16 * ((ks + s) & 3) + 4 * r);
#pragma unroll
      for (int e = 0; e < 8; ++e) d[e] = mfma(af, bfrag(wr + 8 * ks, e), d[e]);
      SB();
    }
#undef LD_WQ
    if (r == 0) {
      *(f32x4*)(lds + L_QP + a * DH + 8 * c) = f32x4{d[0][0], d[1][0], d[2][0], d[3][0]};
      *(f32x4*)(lds + L_QP + a * DH + 8 * c + 4) = f32x4{d[4][0], d[5][0], d[6][0], d[7][0]};
    }
    LGKM0();
    STAMP(6);
    __builtin_amdgcn_s_barrier();
    const unsigned tag = lds[L_TAG];
    const rsrc_t rw = mkrsrc(ws, WS_END);
    u32x4 bf[32];
    {
      const rsrc_t ro = mkrsrc(wo + (size_t)h * DH * DM, DH * DM * 2);
#pragma unroll
      for (int ks = 0; ks < 4; ++ks) {
#pragma unroll
        for (int i = 0; i < 8; ++i) {
          __builtin_amdgcn_s_sleep(WO_SLEEP);
          if (!(ABL & 1)) wr[i] = __builtin_amdgcn_raw_buffer_load_b128(ro, r * 8192 + c * 16 + (i & 3) * 1024, ks * 32768 + (i >> 2) * 4096 + a * 256, OAUX);
          SB();
        }
#pragma unroll
        for (int e = 0; e < 8; ++e) { bf[8 * ks + e] = bfrag(wr, e); asm volatile("" : "+v"(bf[8 * ks + e])); }
        SB();
      }
    }
    if (fgrp && s == 0) {
      const int sl = ((bid & 7) >> 1) * 7 + (j >> 2), f0 = sl * FSL;
      const u32x4 xo = bldc(rx, lane * 16, (b ^ 1) * (DM * 2));
      const GAS u32x4* p13 = (const GAS u32x4*)((c < 8 ? w1 : w3) + (size_t)(128 * a + 8 * r) * FF + f0 + 8 * (c & 7));
#pragma unroll
      for (int i = 0; i < 8; ++i) wr[i] = __builtin_nontemporal_load(p13 + i * (FF / 8));
      SB();
      {
        const u32x4 n1 = layernorm8(xo);
        if (r == a) *(u32x4*)(xn + 64 * (b ^ 1) + 4 * c) = n1;
      }
#pragma unroll
      for (int e = 0; e < 8; ++e) d[e] = f32x4{0.f, 0.f, 0.f, 0.f};
      const rsrc_t r2 = mkrsrc(w2 + (size_t)f0 * DM, FSL * DM * 2);
#pragma unroll
      for (int ks = 0; ks < 4; ++ks) {
        const u32x4 af = *(const u32x4*)(xn + 64 * (c & 1) + 16 * ks + 4 * r);
#pragma unroll
        for (int e = 0; e < 8; ++e) d[e] = mfma(af, bfrag(wr, e), d[e]);
        SB();
#pragma unroll
        for (int jj = 0; jj < 8; ++jj)
          wr[jj] = ks < 3 ? __builtin_nontemporal_load(p13 + (32 * (ks + 1) + jj) * (FF / 8))
                          : bld(r2, r * 8192 + c * 16 + (jj & 3) * 1024, (jj >> 2) * 4096 + a * 256);
        SB();
      }
      if (r < 2) {
        float* gu = (float*)lds + L_GU + a * 256 + r * 128 + 8 * c;
        *(f32x4*)(gu) = r ? f32x4{d[0][1], d[1][1], d[2][1], d[3][1]} : f32x4{d[0][0], d[1][0], d[2][0], d[3][0]};
        *(f32x4*)(gu + 4) = r ? f32x4{d[4][1], d[5][1], d[6][1], d[7][1]} : f32x4{d[4][0], d[5][0], d[6][0], d[7][0]};
      }
      LGKM0();
      if (lane == 0) __hip_atomic_fetch_add((LAS unsigned*)(lds + L_CNT), 1u, __ATOMIC_RELAXED, __HIP_MEMORY_SCOPE_WORKGROUP);
      while (*(volatile LAS unsigned*)(lds + L_CNT) < 4u) __builtin_amdgcn_s_sleep(1);
      asm volatile("" ::: "memory");
      {
        const float* gu = (const float*)lds + L_GU + (lane >> 5) * 128 + 2 * (lane & 31);
        f32x2 g = {0.f, 0.f}, u = {0.f, 0.f};
#pragma unroll
        for (int aa = 0; aa < 4; ++aa) { g += *(const f32x2*)(gu + aa * 256); u += *(const f32x2*)(gu + aa * 256 + 64); }
        float hv[2];
#pragma unroll
        for (int k = 0; k < 2; ++k) { const float gg = rbf(g[k]); hv[k] = rbf(gg / (1.0f + __expf(-gg))) * rbf(u[k]); }
        lds[L_H + a * 64 + lane] = pack2(hv[0], hv[1]);
      }
#pragma unroll
      for (int e = 0; e < 8; ++e) d[e] = f32x4{0.f, 0.f, 0.f, 0.f};
#pragma unroll
      for (int ks = 0; ks < 2; ++ks) {
        const u32x4 af = *(const u32x4*)(lds + L_H + a * 64 + 32 * (c & 1) + 16 * ks + 4 * r);
#pragma unroll
        for (int e = 0; e < 8; ++e) d[e] = mfma(af, bfrag(wr, e), d[e]);
        SB();
        if (ks == 0) {
#pragma unroll
          for (int jj = 0; jj < 8; ++jj) wr[jj] = bld(r2, r * 8192 + c * 16 + (jj & 3) * 1024, 32768 + (jj >> 2) * 4096 + a * 256);
          SB();
        }
      }
      {
        const int fo = WS_F + (((r & 1) * 32 + 8 * a + (c >> 1)) * NFW + sl) * 128 + (c & 1) * 64 + (r >> 1) * 32;
        float t[4];
#pragma unroll
        for (int e = 0; e < 4; ++e) {
          const float p0 = (r & 1) ? d[e][1] : d[e][0], p1 = (r & 1) ? d[4 + e][1] : d[4 + e][0];
          t[e] = r < 2 ? p0 : p1;
        }
        gst(rw, fo, 0, t[0], t[1], tag); gst(rw, fo + 16, 0, t[2], t[3], tag);
      }
      SB();
    }
    if (a == 0) {
      while (*(volatile LAS unsigned*)(lds + L_MID) < 4u) __builtin_amdgcn_s_sleep(16);
      if (!(fgrp && s == 0)) for (int i = 0; i < ML_SLEEP; ++i) __builtin_amdgcn_s_sleep(127);
      u32x4 ml;
      for (unsigned spin = 0; spin < SPIN_CAP; ++spin) {
        asm volatile("" ::: "memory");
        ml = gld(rw, c * 16, WS_ML + (b * NH + h) * 256);
        if (__builtin_amdgcn_ballot_w64(((ml[1] ^ tag) | (ml[3] ^ tag)) != 0u) == 0ull) break;
        __builtin_amdgcn_s_sleep(64);
      }
      const float mw = __uint_as_float(ml[0]), M = row16_max(mw);
      const float e = __builtin_amdgcn_exp2f(mw - M);
      const float f = e / row16_sum(e * __uint_as_float(ml[2]));
      if ((lane >> 2) == s) lds[L_FW + (lane & 3)] = __float_as_uint(f);
      LGKM0();
    }
    STAMP(4);
    __builtin_amdgcn_s_barrier();
    u32x4 af[4];
#pragma unroll
    for (int ks = 0; ks < 4; ++ks) af[ks] = *(const u32x4*)(lds + L_A + (ks * 64 + r * 16 + (c & 7)) * 4);
    const f32x2 fw = *(const f32x2*)(lds + L_FW + 2 * (r & 1));
    const float f0 = fw[0], f1 = fw[1];
#pragma unroll
    for (int e = 0; e < 8; ++e) d[e] = f32x4{0.f, 0.f, 0.f, 0.f};
#pragma unroll
    for (int ks = 0; ks < 4; ++ks)
#pragma unroll
      for (int e = 0; e < 8; ++e) d[e] = mfma(af[ks], bf[8 * ks + e], d[e]);
    float v[4];
#pragma unroll
    for (int e = 0; e < 4; ++e) {
      const float p0 = d[e][0] + d[e][1], p1 = d[e][2] + d[e][3], q0 = d[4 + e][0] + d[4 + e][1], q1 = d[4 + e][2] + d[4 + e][3];
      v[e] = f0 * (r < 2 ? p0 : q0) + f1 * (r < 2 ? p1 : q1);
    }
    gst(rw, (c >> 1) * (128 * 128) + (c & 1) * 64 + r * 16, WS_Y + ((b * 32 + 8 * a) * 128 + h * NSPLIT + s) * 128,
        t16_sum(v[0], v[2]), t16_sum(v[1], v[3]), tag);
    STAMP(3); STAMP_FLUSH;
    return;
  }

  u32x4 ring[16 * (1 + NXR)];
  const int vo = lane * 16;
  const int lim = nst * STEPB;
  const rsrc_t rk = mkrsrc((const char*)kc + (unsigned)kvb, lim), rv = mkrsrc((const char*)vc + (unsigned)kvb, lim);
  int so = so0;
  const int tstart = (int)__builtin_amdgcn_s_memrealtime();
  int dt = ((T1 + LEAD) * 64) / nst, tg = -(LEAD + (fgrp && s == 0 ? FFN_AHEAD : 0)) * 64;
#define PACE(g) if ((g) % PGR == 0) { while (((int)__builtin_amdgcn_s_memrealtime() - tstart) * 64 < tg) __builtin_amdgcn_s_sleep(PSL); } tg += dt;
#define ADV() so += SMUL * STEPB; so = so >= lim ? so - lim : so;
#define FILL(rs, g, o) _Pragma("unroll") for (int i = 0; i < 4; ++i) { ring[(g) * 4 + i] = bld(rs, vo + i * 1024, o); SB(); }
#pragma unroll
  for (int g = 4; g < 4 + 4 * NXR; ++g) { if (PB && g - 4 >= PB) { PACE(0) } else tg += dt; FILL(rk, g, so) if (g == 4) STAMP(5); ADV() }
#pragma unroll
  for (int g = 0; g < 4; ++g) { if (PB) { PACE(0) } else tg += dt; FILL(rk, g, so) ADV() }
  STAMP(6);
  __builtin_amdgcn_s_barrier();
  unsigned qp[4];
  {
    f32x4 t0 = *(const f32x4*)(lds + L_QP + 8 * c), t1 = *(const f32x4*)(lds + L_QP + 8 * c + 4);
#pragma unroll
    for (int aa = 1; aa < 4; ++aa) {
      t0 += *(const f32x4*)(lds + L_QP + aa * DH + 8 * c);
      t1 += *(const f32x4*)(lds + L_QP + aa * DH + 8 * c + 4);
    }
    qp[0] = pack2(t0[0], t0[1]); qp[1] = pack2(t0[2], t0[3]); qp[2] = pack2(t1[0], t1[1]); qp[3] = pack2(t1[2], t1[3]);
  }
  STAMP(1);
  constexpr float SCALE = 0.088388347648318440f, LOG2E = 1.4426950408889634f;
  float* sl = (float*)lds + L_S + w * (NREVMAX * 64) + lane;
  float m = -INFINITY;
  float xs[16];
#define QK(g, slot)                                                                                  \
    _Pragma("unroll") for (int i = 0; i < 4; ++i) {                                                  \
      float d_ = 0.f;                                                                                \
      _Pragma("unroll") for (int k = 0; k < 4; ++k) d_ = dot2(ring[(slot) * 4 + i][k], qp[k], d_);   \
      xs[(g) * 4 + i] = d_;                                                                          \
    }                                                                                                \
    PIN4(xs + (g) * 4);
#define TR(HALF, CTRL) _Pragma("unroll") for (int k = 0; k < (HALF); ++k) {                          \
      const float t1_ = xs[k] + dpp<CTRL>(xs[k]), t2_ = xs[k + (HALF)] + dpp<CTRL>(xs[k + (HALF)]);  \
      xs[k] = (c & (HALF)) ? t2_ : t1_; }
#define SCORE(rev, nvalid)                                                                           \
  {                                                                                                  \
    TR(8, XOR8) TR(4, XOR7) TR(2, XOR2) TR(1, XOR1)                                                  \
    float t = rbf(rbf(xs[0]) * SCALE) * LOG2E;                     \
    t = (c >> 2) < (nvalid) ? t : -INFINITY;                                                         \
    m = fmaxf(m, t);                                                                                 \
    sl[(rev) * 64] = t;                                                                              \
    SB();                                                                                            \
  }
  const int nrev = (nst + 3) >> 2;
  {
    int left = nst - 4 * (1 + NXR);
    rsrc_t rn = rk;
#pragma nounroll
    for (int rev = NXR; rev < nrev; ++rev) {
      if (rev == nrev - 1) { rn = rv; so = so0; left = 4; tg = T1 * 64; dt = ((T2 - T1 - (fgrp && s == 0 ? FFN_AHEAD2 : (bid & 1) == 0 && j < 16 ? CONS_AHEAD : (bid & 1) == 0 ? EVEN_AHEAD : 0)) * 64) / nst; }
#define STEP(g) { const int o_ = left > (g) ? so : lim; QK(g, g) PACE(g) FILL(rn, g, o_) ADV() }
      STEP(0) STEP(1) STEP(2) STEP(3)
#undef STEP
      SCORE(rev, nst - 4 * rev)
      left -= 4;
#define XREV(e) if constexpr ((e) < NXR) { if (rev == NXR + (e)) {                                   \
        _Pragma("unroll") for (int g = 0; g < 4; ++g) { PIN4(ring + (4 * (1 + (e)) + g) * 4); QK(g, 4 * (1 + (e)) + g) } \
        SCORE(e, 4) } }
      XREV(0) XREV(1)
#undef XREV
    }
  }
  {
    m = xrow_max(row16_max(m));
    float l = 0.f;
#pragma unroll
    for (int rev = 0; rev < NREVMAX; ++rev) {
      const float t = rev < NREVMAX - 3 || rev < nrev ? sl[rev * 64] : -INFINITY;
      const float p = __builtin_amdgcn_exp2f(t - m);
      l += p;
      sl[rev * 64] = p;
    }
    l = wave_sum(l);
    gst(mkrsrc(ws, WS_END), lane == 0 ? 0 : WS_END, WS_ML + (b * NH + h) * 256 + (4 * s + w) * 16, m, l, lds[L_TAG]);
    if (lane == 0) __hip_atomic_fetch_add((LAS unsigned*)(lds + L_MID), 1u, __ATOMIC_RELAXED, __HIP_MEMORY_SCOPE_WORKGROUP);
    SB();
  }
  STAMP(3);
  f32x2 acc[4];
#pragma unroll
  for (int k = 0; k < 4; ++k) acc[k] = f32x2{0.f, 0.f};
#define PV1(g, i) { const float pb = dpp<BCAST + 4 * (g) + (i)>(p); const u32x4 vv = ring[(g) * 4 + (i)];  \
      _Pragma("unroll") for (int k = 0; k < 4; ++k)                                                  \
        acc[k] = __builtin_elementwise_fma(f32x2{pb, pb}, f32x2{blo(vv[k]), bhi(vv[k])}, acc[k]); }
#define PV(g) PV1(g, 0) PV1(g, 1) PV1(g, 2) PV1(g, 3) PIN4(acc);
  int left = nst - 4;
  float pn = sl[0];
#pragma nounroll
  for (int rev = 1; rev < nrev; ++rev) {
    const float p = pn;
    pn = sl[rev * 64];
#define STEP(g) { const int o_ = left > (g) ? so : lim; PV(g) if (left > (g) + SPRINT) { PACE(g) } FILL(rv, g, o_) ADV() }
    STEP(0) STEP(1) STEP(2) STEP(3)
#undef STEP
    left -= 4;
  }
  {
    const float p = pn;
    PV(0)
    if (left > -3) { PV(1) }
    if (left > -2) { PV(2) }
    if (left > -1) { PV(3) }
  }
  STAMP(2);
  {
    const float a0 = t32_sum(acc[0][0], acc[2][0]), a1 = t32_sum(acc[0][1], acc[2][1]);
    const float a2 = t32_sum(acc[1][0], acc[3][0]), a3 = t32_sum(acc[1][1], acc[3][1]);
    const float t0 = t16_sum(a0, a2), t1 = t16_sum(a1, a3);
    const unsigned hi = pack2(t0, t1), lo = pack2(t0 - blo(hi), t1 - bhi(hi));
    unsigned* pa = lds + L_A + ((c >> 2) * 64 + (c & 3) * 16 + 2 * w) * 4 + r;
    pa[0] = hi; pa[4] = lo;
  }
  LGKM0();
  STAMP(4);
  __builtin_amdgcn_s_barrier();
  if (!(ABL & 6) && (bid & 1) == CONS_PAR && j < 16) {
    const int ci = ((bid & 7) >> 1) * 16 + j, cb = ci >> 5, cg = ci & 31;
    const int t = tid - 256, pc = t & 7, hh = t >> 3;
    const unsigned tag = lds[L_TAG];
    const rsrc_t rw = mkrsrc(ws, WS_END);
    const int sy = WS_Y + (cb * 32 + cg) * (128 * 128);
    int vy = hh * 512 + pc * 16;
    int vf = hh < NFW ? WS_F + ((cb * 32 + cg) * NFW + hh) * 128 + pc * 16 : sy + vy;
    int vx = ci == 0 ? WS_ML + NH * 256 + (t & 127) * 64 : vf;
    const unsigned xv = *(const unsigned*)(x + cb * DM + cg * 16 + 2 * pc);
    float k0 = 0.f, k1 = 0.f, g0 = 0.f, g1 = 0.f, a0 = 0.f;
    bool done = false;
    u32x4 ya[5], yb[5];
    unsigned yat, ybt;
#define SWEEP(Y) { _Pragma("unroll") for (int i = 0; i < 4; ++i) Y[i] = gld(rw, vy + i * 128, sy);    \
      Y[4] = gld(rw, vf, 0); Y##t = __builtin_amdgcn_raw_buffer_load_b32(rw, vx, 4, 16); asm volatile("" ::: "memory"); SB(); }
#define CHECK(Y) {                                                                                   \
      unsigned bad = Y##t ^ tag;                                                                    \
      _Pragma("unroll") for (int i = 0; i < 5; ++i) bad |= (Y[i][1] ^ tag) | (Y[i][3] ^ tag);        \
      const bool take = !done && bad == 0u;                                                          \
      k0 = take ? (__uint_as_float(Y[0][0]) + __uint_as_float(Y[1][0])) + (__uint_as_float(Y[2][0]) + __uint_as_float(Y[3][0])) : k0; \
      k1 = take ? (__uint_as_float(Y[0][2]) + __uint_as_float(Y[1][2])) + (__uint_as_float(Y[2][2]) + __uint_as_float(Y[3][2])) : k1; \
      g0 = take && hh < NFW ? __uint_as_float(Y[4][0]) : g0; g1 = take && hh < NFW ? __uint_as_float(Y[4][2]) : g1; \
      done = done || take;                                                                           \
      vy = done ? WS_END : vy; vf = done ? WS_END : vf; vx = done ? WS_END : vx;                     \
      a0 = t32_sum(k0 + dpp<XOR8>(k0), g0 + dpp<XOR8>(g0));        \
      a0 = t16_sum(a0, t32_sum(k1 + dpp<XOR8>(k1), g1 + dpp<XOR8>(g1)));     \
      const unsigned long long miss = __builtin_amdgcn_ballot_w64(!done);                            \
      if (miss == 0ull) break;                                                                       \
      if (__builtin_popcountll(miss) > POLL_MANY) __builtin_amdgcn_s_sleep(POLL_SLEEP);              \
      asm volatile("" ::: "memory"); }
    SWEEP(ya)
    __builtin_amdgcn_s_sleep(12);
    for (unsigned spin = 0; spin < SPIN_CAP; ++spin) { SWEEP(yb) CHECK(ya) SWEEP(ya) CHECK(yb) }
#undef SWEEP
#undef CHECK
    if (c < 8) lds[L_RED + w * 32 + (r >> 1) * 16 + 2 * c + (r & 1)] = __float_as_uint(a0);
    LGKM0();
    unsigned tk = 0;
    if (lane == 0) tk = __hip_atomic_fetch_add((LAS unsigned*)(lds + L_CNT), 16u, __ATOMIC_RELAXED, __HIP_MEMORY_SCOPE_WORKGROUP);
    if (__builtin_amdgcn_readfirstlane(tk) >= 48u) {
      asm volatile("" ::: "memory");
      if (lane < 8) {
        const float* red = (const float*)lds + L_RED + 2 * lane;
        const f32x2 A = (*(const f32x2*)(red) + *(const f32x2*)(red + 32)) + (*(const f32x2*)(red + 64) + *(const f32x2*)(red + 96));
        const f32x2 F = (*(const f32x2*)(red + 16) + *(const f32x2*)(red + 48)) + (*(const f32x2*)(red + 80) + *(const f32x2*)(red + 112));
        *(unsigned*)(out + cb * DM + cg * 16 + 2 * lane) = pack2(rbf(blo(xv) + rbf(A[0])) + rbf(F[0]), rbf(bhi(xv) + rbf(A[1])) + rbf(F[1]));
      }
      if (ci == 0 && lane == 0) __builtin_amdgcn_raw_buffer_store_b32(tag, rw, WS_E, 0, 16);
    }
    STAMP(7);
    asm volatile("" ::"v"(ya[0]), "v"(ya[1]), "v"(ya[2]), "v"(ya[3]), "v"(ya[4]), "v"(yat), "v"(yb[0]), "v"(yb[1]), "v"(yb[2]), "v"(yb[3]), "v"(yb[4]), "v"(ybt) : "memory");
    STAMP_FLUSH;
    return;
  }
  STAMP(7);
  STAMP_FLUSH;
}

}

extern "C" void kernel_launch(void* const* d_in, const int* in_sizes, int n_in, void* d_out, int out_size, void* d_ws,
                              size_t ws_size, hipStream_t stream) {
  attn_kernel<<<NWG, NTHR, 0, stream>>>((const u16*)d_in[1], (const u16*)d_in[2], (const u16*)d_in[0], (const u16*)d_in[3], (unsigned*)d_ws,
                                        (u16*)d_out, (const u16*)d_in[4], (const u16*)d_in[8], (const u16*)d_in[6], (const u16*)d_in[7]);
}
```

```cpp
#include <hip/hip_runtime.h>
#include <hip/hip_bf16.h>
#include <cstdint>

namespace {

constexpr int NB = 2, DM = 512, NH = 32, SK = 16384, DH = 128, FF = 1792;
constexpr int NSPLIT = 4;
constexpr int NTHR = 512;
constexpr int FSH = 2;
constexpr int NXR = 2;
constexpr int T1 = 3900, T2 = 7550, AH_FFN1 = 300, AH_FFN2 = 100, AH_CONS = 80;
constexpr int WQ_INFL = 8;
constexpr int NWG = NB * NH * NSPLIT;
constexpr int STEPK = 64, STEPB = STEPK * DH * 2;
constexpr int NSTEP = SK / NSPLIT / STEPK;
constexpr int NREVMAX = (NSTEP + FSH + 3) / 4;
constexpr int FSL = 64, NFW = FF / FSL;

constexpr int WS_E = 0;
constexpr int WS_ML = 4096;
constexpr int WS_F = WS_ML + NB * NH * 256;
constexpr int WS_Y = WS_F + NB * 32 * NFW * 128;
constexpr int WS_END = WS_Y + NB * 32 * 128 * 128;
constexpr unsigned SPIN_CAP = 1u << 20;

typedef unsigned u32x4 __attribute__((ext_vector_type(4)));
typedef float f32x4 __attribute__((ext_vector_type(4)));
typedef float f32x2 __attribute__((ext_vector_type(2)));
typedef __bf16 bf16x2 __attribute__((ext_vector_type(2)));
typedef __bf16 bf16x8 __attribute__((ext_vector_type(8)));
typedef unsigned short u16;
#define GAS __attribute__((address_space(1)))
#define LAS __attribute__((address_space(3)))

__device__ __forceinline__ float dot2(unsigned a, unsigned b, float acc) {
  return __builtin_amdgcn_fdot2_f32_bf16(__builtin_bit_cast(bf16x2, a), __builtin_bit_cast(bf16x2, b), acc, false);
}
__device__ __forceinline__ float blo(unsigned u) { return __builtin_bit_cast(float, u << 16); }
__device__ __forceinline__ float bhi(unsigned u) { return __builtin_bit_cast(float, u & 0xffff0000u); }
__device__ __forceinline__ float rbf(float f) { return (float)(__bf16)f; }
__device__ __forceinline__ unsigned pack2(float lo, float hi) {
  bf16x2 v; v.x = (__bf16)lo; v.y = (__bf16)hi; return __builtin_bit_cast(unsigned, v);
}
template <int CTRL> __device__ __forceinline__ float dpp(float x) {
  return __builtin_bit_cast(float, __builtin_amdgcn_mov_dpp(__builtin_bit_cast(int, x), CTRL, 0xf, 0xf, true));
}
constexpr int XOR1 = 0xB1, XOR2 = 0x4E, XOR7 = 0x141, XOR8 = 0x128, BCAST = 0x150;
__device__ __forceinline__ float row16_sum(float x) {
  x += dpp<XOR1>(x); x += dpp<XOR2>(x); x += dpp<XOR7>(x); x += dpp<XOR8>(x); return x;
}
__device__ __forceinline__ float row16_max(float x) {
  x = fmaxf(x, dpp<XOR1>(x)); x = fmaxf(x, dpp<XOR2>(x)); x = fmaxf(x, dpp<XOR7>(x)); return fmaxf(x, dpp<XOR8>(x));
}
__device__ __forceinline__ float t16_sum(float a, float b) {
  auto s = __builtin_amdgcn_permlane16_swap(__float_as_uint(a), __float_as_uint(b), false, false);
  return __uint_as_float(s[0]) + __uint_as_float(s[1]);
}
__device__ __forceinline__ float t32_sum(float a, float b) {
  auto s = __builtin_amdgcn_permlane32_swap(__float_as_uint(a), __float_as_uint(b), false, false);
  return __uint_as_float(s[0]) + __uint_as_float(s[1]);
}
__device__ __forceinline__ float xrow_sum(float x) { x = t16_sum(x, x); return t32_sum(x, x); }
__device__ __forceinline__ float xrow_max(float x) {
  auto s = __builtin_amdgcn_permlane16_swap(__float_as_uint(x), __float_as_uint(x), false, false);
  x = fmaxf(__uint_as_float(s[0]), __uint_as_float(s[1]));
  auto t = __builtin_amdgcn_permlane32_swap(__float_as_uint(x), __float_as_uint(x), false, false);
  return fmaxf(__uint_as_float(t[0]), __uint_as_float(t[1]));
}
__device__ __forceinline__ float wave_sum(float x) { return xrow_sum(row16_sum(x)); }
typedef __amdgpu_buffer_rsrc_t rsrc_t;
__device__ __forceinline__ rsrc_t mkrsrc(const void* p, int bytes) { return __builtin_amdgcn_make_buffer_rsrc((void*)p, 0, bytes, 0x00020000); }
__device__ __forceinline__ u32x4 bld(rsrc_t r, int voff, int soff) { return __builtin_amdgcn_raw_buffer_load_b128(r, voff, soff, 2); }
__device__ __forceinline__ u32x4 bldc(rsrc_t r, int voff, int soff) { return __builtin_amdgcn_raw_buffer_load_b128(r, voff, soff, 0); }
__device__ __forceinline__ u32x4 gld(rsrc_t r, int voff, int soff) { return __builtin_amdgcn_raw_buffer_load_b128(r, voff, soff, 16); }
__device__ __forceinline__ void gst(rsrc_t r, int voff, int soff, float v0, float v1, unsigned tag) {
  __builtin_amdgcn_raw_buffer_store_b128(u32x4{__float_as_uint(v0), tag, __float_as_uint(v1), tag}, r, voff, soff, 16);
}
#define SB() __builtin_amdgcn_sched_barrier(0)
__device__ __forceinline__ unsigned load_tag(const unsigned* ws) {
  unsigned e;
  asm volatile("s_load_dword %0, %1, 0x0\n\ts_waitcnt lgkmcnt(0)" : "=s"(e) : "s"(ws) : "memory");
  return e + 1u;
}
#define PIN4(a) asm volatile("" : "+v"((a)[0]), "+v"((a)[1]), "+v"((a)[2]), "+v"((a)[3])::"memory"); SB()
#define LGKM0() asm volatile("s_waitcnt lgkmcnt(0)" ::: "memory")
#define CFENCE() asm volatile("" ::: "memory")
#define BARRIER() do { LGKM0(); __builtin_amdgcn_s_barrier(); CFENCE(); } while (0)
__device__ __forceinline__ f32x4 mfma(u32x4 a, u32x4 b, f32x4 c) {
  return __builtin_amdgcn_mfma_f32_16x16x32_bf16(__builtin_bit_cast(bf16x8, a), __builtin_bit_cast(bf16x8, b), c, 0, 0, 0);
}
__device__ __forceinline__ u32x4 bfrag(const u32x4* w, int e) {
  u32x4 o;
#pragma unroll
  for (int jj = 0; jj < 4; ++jj) o[jj] = __builtin_amdgcn_perm(w[2 * jj + 1][e >> 1], w[2 * jj][e >> 1], (e & 1) ? 0x07060302u : 0x05040100u);
  return o;
}
__device__ __forceinline__ u32x4 layernorm8(u32x4 xa) {
  float e[8];
#pragma unroll
  for (int k = 0; k < 4; ++k) { e[2 * k] = blo(xa[k]); e[2 * k + 1] = bhi(xa[k]); }
  const float mean = wave_sum(((e[0] + e[1]) + (e[2] + e[3])) + ((e[4] + e[5]) + (e[6] + e[7]))) * (1.0f / DM);
  float q = 0.f;
#pragma unroll
  for (int k = 0; k < 8; ++k) { const float d = e[k] - mean; q = fmaf(d, d, q); }
  const float var = wave_sum(q) * (1.0f / DM);
  const float mb = rbf(mean), sd = rbf(sqrtf(rbf(rbf(var) + 1e-5f)));
  const float ri = 1.0f / sd;
  u32x4 o;
#pragma unroll
  for (int k = 0; k < 4; ++k) {
    const float d0 = rbf(e[2 * k] - mb), d1 = rbf(e[2 * k + 1] - mb);
    float q0 = d0 * ri, q1 = d1 * ri;
    q0 = fmaf(fmaf(-q0, sd, d0), ri, q0); q1 = fmaf(fmaf(-q1, sd, d1), ri, q1);
    o[k] = pack2(q0, q1);
  }
  return o;
}

constexpr int L_QP = 0;
constexpr int L_A = L_QP + 4 * DH;
constexpr int L_CNT = L_A + 1024, L_TAG = L_CNT + 1, L_MID = L_CNT + 2, L_FW = L_CNT + 4;
constexpr int L_RED = L_CNT + 16;
constexpr int L_XN = L_RED + 128;
constexpr int L_GU = L_XN + 4 * 128;
constexpr int L_H = L_GU + 4 * 256;
constexpr int L_S = L_H + 4 * 64;
constexpr int L_TOT = L_S + 4 * NREVMAX * 64;

__global__ void __launch_bounds__(NTHR, 2) attn_kernel(const u16* __restrict__ kc, const u16* __restrict__ vc, const u16* __restrict__ x,
                                                       const u16* __restrict__ wq, unsigned* ws, u16* __restrict__ out,
                                                       const u16* __restrict__ wo, const u16* __restrict__ w2,
                                                       const u16* __restrict__ w1, const u16* __restrict__ w3) {
  __shared__ __attribute__((aligned(16))) unsigned lds[L_TOT];
  const int bid = blockIdx.x;
  const int tid = threadIdx.x, lane = tid & 63, wv = __builtin_amdgcn_readfirstlane(tid >> 6);
  const int j = bid >> 3, h = (bid & 7) * 4 + (j >> 3), b = (j >> 2) & 1, s = j & 3;
  const int r = lane >> 4, c = lane & 15;
  const bool fgrp = (bid & 1) == 0 && (j >> 2) < 7;
  const int w = wv - 4;
  const int fs = fgrp ? FSH : 0;
  int nst = NSTEP + (s == 0 ? -3 * fs : fs);
  int kvb = ((b * NH + h) * NSPLIT * NSTEP + s * NSTEP + (s == 0 ? 0 : (s - 4) * fs)) * STEPB;
  int so0 = w * 4096 + ((2 * s + b) * 8 + (s == 0 ? 0 : (4 - s) * fs)) * STEPB;
  asm volatile("" : "+s"(nst), "+s"(kvb), "+s"(so0) : "s"(kc), "s"(vc), "s"(x), "s"(wq), "s"(ws), "s"(out));

  if (wv < 4) {
    const int a = wv;
    __builtin_amdgcn_s_setprio(3);
    const rsrc_t rx = mkrsrc(x, NB * DM * 2);
    const u32x4 xa = bldc(rx, lane * 16, b * (DM * 2));
    u32x4 wr[32];
    const rsrc_t rq = mkrsrc(wq + h * DH, (DM * NH * DH - h * DH) * 2);
    const int vq = r * (8 * NH * DH * 2) + c * 16;
#define LD_WQ(i) { if ((i) >= WQ_INFL) asm volatile("s_waitcnt vmcnt(%0)" ::"n"(WQ_INFL - 1) : "memory"); \
      wr[i] = bldc(rq, vq, (128 * a + 32 * ((((i) >> 3) + s) & 3) + ((i) & 7)) * (NH * DH * 2)); SB(); }
#pragma unroll
    for (int i = 0; i < WQ_INFL; ++i) LD_WQ(i)
    if (a == 0) {
      const unsigned t = load_tag(ws);
      if (lane == 0) { lds[L_CNT] = 0u; lds[L_TAG] = t; lds[L_MID] = 0u; }
    }
    SB();
    unsigned* xn = lds + L_XN + a * 128;
    {
      const u32x4 n0 = layernorm8(xa);
      if (r == a) *(u32x4*)(xn + 64 * b + 4 * c) = n0;
    }
    CFENCE(); SB();
    f32x4 d[8];
#pragma unroll
    for (int e = 0; e < 8; ++e) d[e] = f32x4{0.f, 0.f, 0.f, 0.f};
#pragma unroll
    for (int ks = 0; ks < 4; ++ks) {
#pragma unroll
      for (int i = 8 * ks + WQ_INFL; i < 8 * ks + 8 + WQ_INFL; ++i) if (i < 32) LD_WQ(i)
      const u32x4 af = *(const u32x4*)(xn + 64 * b + 16 * ((ks + s) & 3) + 4 * r);
#pragma unroll
      for (int e = 0; e < 8; ++e) d[e] = mfma(af, bfrag(wr + 8 * ks, e), d[e]);
      SB();
    }
#undef LD_WQ
    if (r == 0) {
      *(f32x4*)(lds + L_QP + a * DH + 8 * c) = f32x4{d[0][0], d[1][0], d[2][0], d[3][0]};
      *(f32x4*)(lds + L_QP + a * DH + 8 * c + 4) = f32x4{d[4][0], d[5][0], d[6][0], d[7][0]};
    }
    BARRIER();
    const unsigned tag = lds[L_TAG];
    const rsrc_t rw = mkrsrc(ws, WS_END);
    u32x4 bf[32];
    {
      const rsrc_t ro = mkrsrc(wo + (size_t)h * DH * DM, DH * DM * 2);
#pragma unroll
      for (int ks = 0; ks < 4; ++ks) {
#pragma unroll
        for (int i = 0; i < 8; ++i) {
          __builtin_amdgcn_s_sleep(8);
          wr[i] = bldc(ro, r * 8192 + c * 16 + (i & 3) * 1024, ks * 32768 + (i >> 2) * 4096 + a * 256);
          SB();
        }
#pragma unroll
        for (int e = 0; e < 8; ++e) { bf[8 * ks + e] = bfrag(wr, e); asm volatile("" : "+v"(bf[8 * ks + e])); }
        SB();
      }
    }
    if (fgrp && s == 0) {
      const int sl = ((bid & 7) >> 1) * 7 + (j >> 2), f0 = sl * FSL;
      const u32x4 xo = bldc(rx, lane * 16, (b ^ 1) * (DM * 2));
      const GAS u32x4* p13 = (const GAS u32x4*)((c < 8 ? w1 : w3) + (size_t)(128 * a + 8 * r) * FF + f0 + 8 * (c & 7));
#pragma unroll
      for (int i = 0; i < 8; ++i) wr[i] = __builtin_nontemporal_load(p13 + i * (FF / 8));
      SB();
      {
        const u32x4 n1 = layernorm8(xo);
        if (r == a) *(u32x4*)(xn + 64 * (b ^ 1) + 4 * c) = n1;
      }
      CFENCE();
#pragma unroll
      for (int e = 0; e < 8; ++e) d[e] = f32x4{0.f, 0.f, 0.f, 0.f};
      const rsrc_t r2 = mkrsrc(w2 + (size_t)f0 * DM, FSL * DM * 2);
#pragma unroll
      for (int ks = 0; ks < 4; ++ks) {
        const u32x4 af = *(const u32x4*)(xn + 64 * (c & 1) + 16 * ks + 4 * r);
#pragma unroll
        for (int e = 0; e < 8; ++e) d[e] = mfma(af, bfrag(wr, e), d[e]);
        SB();
#pragma unroll
        for (int jj = 0; jj < 8; ++jj)
          wr[jj] = ks < 3 ? __builtin_nontemporal_load(p13 + (32 * (ks + 1) + jj) * (FF / 8))
                          : bld(r2, r * 8192 + c * 16 + (jj & 3) * 1024, (jj >> 2) * 4096 + a * 256);
        SB();
      }
      if (r < 2) {
        float* gu = (float*)lds + L_GU + a * 256 + r * 128 + 8 * c;
        *(f32x4*)(gu) = r ? f32x4{d[0][1], d[1][1], d[2][1], d[3][1]} : f32x4{d[0][0], d[1][0], d[2][0], d[3][0]};
        *(f32x4*)(gu + 4) = r ? f32x4{d[4][1], d[5][1], d[6][1], d[7][1]} : f32x4{d[4][0], d[5][0], d[6][0], d[7][0]};
      }
      LGKM0();
      if (lane == 0) __hip_atomic_fetch_add((LAS unsigned*)(lds + L_CNT), 1u, __ATOMIC_RELAXED, __HIP_MEMORY_SCOPE_WORKGROUP);
      while (*(volatile LAS unsigned*)(lds + L_CNT) < 4u) __builtin_amdgcn_s_sleep(1);
      CFENCE();
      {
        const float* gu = (const float*)lds + L_GU + (lane >> 5) * 128 + 2 * (lane & 31);
        f32x2 g = {0.f, 0.f}, u = {0.f, 0.f};
#pragma unroll
        for (int aa = 0; aa < 4; ++aa) { g += *(const f32x2*)(gu + aa * 256); u += *(const f32x2*)(gu + aa * 256 + 64); }
        float hv[2];
#pragma unroll
        for (int k = 0; k < 2; ++k) { const float gg = rbf(g[k]); hv[k] = rbf(gg / (1.0f + __expf(-gg))) * rbf(u[k]); }
        lds[L_H + a * 64 + lane] = pack2(hv[0], hv[1]);
      }
      CFENCE();
#pragma unroll
      for (int e = 0; e < 8; ++e) d[e] = f32x4{0.f, 0.f, 0.f, 0.f};
#pragma unroll
      for (int ks = 0; ks < 2; ++ks) {
        const u32x4 af = *(const u32x4*)(lds + L_H + a * 64 + 32 * (c & 1) + 16 * ks + 4 * r);
#pragma unroll
        for (int e = 0; e < 8; ++e) d[e] = mfma(af, bfrag(wr, e), d[e]);
        SB();
        if (ks == 0) {
#pragma unroll
          for (int jj = 0; jj < 8; ++jj) wr[jj] = bld(r2, r * 8192 + c * 16 + (jj & 3) * 1024, 32768 + (jj >> 2) * 4096 + a * 256);
          SB();
        }
      }
      {
        const int fo = WS_F + (((r & 1) * 32 + 8 * a + (c >> 1)) * NFW + sl) * 128 + (c & 1) * 64 + (r >> 1) * 32;
        float t[4];
#pragma unroll
        for (int e = 0; e < 4; ++e) {
          const float p0 = (r & 1) ? d[e][1] : d[e][0], p1 = (r & 1) ? d[4 + e][1] : d[4 + e][0];
          t[e] = r < 2 ? p0 : p1;
        }
        gst(rw, fo, 0, t[0], t[1], tag); gst(rw, fo + 16, 0, t[2], t[3], tag);
      }
      SB();
    }
    if (a == 0) {
      while (*(volatile LAS unsigned*)(lds + L_MID) < 4u) __builtin_amdgcn_s_sleep(16);
      if (!(fgrp && s == 0)) for (int i = 0; i < 3; ++i) __builtin_amdgcn_s_sleep(127);
      u32x4 ml;
      for (unsigned spin = 0; spin < SPIN_CAP; ++spin) {
        CFENCE();
        ml = gld(rw, c * 16, WS_ML + (b * NH + h) * 256);
        if (__builtin_amdgcn_ballot_w64(((ml[1] ^ tag) | (ml[3] ^ tag)) != 0u) == 0ull) break;
        __builtin_amdgcn_s_sleep(64);
      }
      const float mw = __uint_as_float(ml[0]), M = row16_max(mw);
      const float e = __builtin_amdgcn_exp2f(mw - M);
      const float f = e / row16_sum(e * __uint_as_float(ml[2]));
      if ((lane >> 2) == s) lds[L_FW + (lane & 3)] = __float_as_uint(f);
    }
    BARRIER();
    u32x4 af[4];
#pragma unroll
    for (int ks = 0; ks < 4; ++ks) af[ks] = *(const u32x4*)(lds + L_A + (ks * 64 + r * 16 + (c & 7)) * 4);
    const f32x2 fw = *(const f32x2*)(lds + L_FW + 2 * (r & 1));
    const float f0 = fw[0], f1 = fw[1];
#pragma unroll
    for (int e = 0; e < 8; ++e) d[e] = f32x4{0.f, 0.f, 0.f, 0.f};
#pragma unroll
    for (int ks = 0; ks < 4; ++ks)
#pragma unroll
      for (int e = 0; e < 8; ++e) d[e] = mfma(af[ks], bf[8 * ks + e], d[e]);
    float v[4];
#pragma unroll
    for (int e = 0; e < 4; ++e) {
      const float p0 = d[e][0] + d[e][1], p1 = d[e][2] + d[e][3], q0 = d[4 + e][0] + d[4 + e][1], q1 = d[4 + e][2] + d[4 + e][3];
      v[e] = f0 * (r < 2 ? p0 : q0) + f1 * (r < 2 ? p1 : q1);
    }
    gst(rw, (c >> 1) * (128 * 128) + (c & 1) * 64 + r * 16, WS_Y + ((b * 32 + 8 * a) * 128 + h * NSPLIT + s) * 128,
        t16_sum(v[0], v[2]), t16_sum(v[1], v[3]), tag);
    return;
  }

  u32x4 ring[16 * (1 + NXR)];
  const int vo = lane * 16;
  const int lim = nst * STEPB;
  const rsrc_t rk = mkrsrc((const char*)kc + (unsigned)kvb, lim), rv = mkrsrc((const char*)vc + (unsigned)kvb, lim);
  int so = so0;
  const bool ffn = fgrp && s == 0, cons = (bid & 1) == 0 && j < 16;
  const int tstart = (int)__builtin_amdgcn_s_memrealtime();
  int dt = (T1 * 64) / nst, tg = dt * 4 * (1 + NXR) - (ffn ? AH_FFN1 * 64 : 0);
#define PACE() { while (((int)__builtin_amdgcn_s_memrealtime() - tstart) * 64 < tg) __builtin_amdgcn_s_sleep(1); tg += dt; }
#define ADV() so += STEPB; so = so >= lim ? so - lim : so;
#define FILL(rs, g, o) _Pragma("unroll") for (int i = 0; i < 4; ++i) { ring[(g) * 4 + i] = bld(rs, vo + i * 1024, o); SB(); }
#pragma unroll
  for (int g = 4; g < 4 + 4 * NXR; ++g) { FILL(rk, g, so) ADV() }
#pragma unroll
  for (int g = 0; g < 4; ++g) { FILL(rk, g, so) ADV() }
  BARRIER();
  unsigned qp[4];
  {
    f32x4 t0 = *(const f32x4*)(lds + L_QP + 8 * c), t1 = *(const f32x4*)(lds + L_QP + 8 * c + 4);
#pragma unroll
    for (int aa = 1; aa < 4; ++aa) {
      t0 += *(const f32x4*)(lds + L_QP + aa * DH + 8 * c);
      t1 += *(const f32x4*)(lds + L_QP + aa * DH + 8 * c + 4);
    }
    qp[0] = pack2(t0[0], t0[1]); qp[1] = pack2(t0[2], t0[3]); qp[2] = pack2(t1[0], t1[1]); qp[3] = pack2(t1[2], t1[3]);
  }
  constexpr float SCALE = 0.088388347648318440f, LOG2E = 1.4426950408889634f;
  float* sl = (float*)lds + L_S + w * (NREVMAX * 64) + lane;
  float m = -INFINITY;
  float xs[16];
#define QK(g, slot)                                                                                  \
    _Pragma("unroll") for (int i = 0; i < 4; ++i) {                                                  \
      float d_ = 0.f;                                                                                \
      _Pragma("unroll") for (int k = 0; k < 4; ++k) d_ = dot2(ring[(slot) * 4 + i][k], qp[k], d_);   \
      xs[(g) * 4 + i] = d_;                                                                          \
    }                                                                                                \
    PIN4(xs + (g) * 4);
#define TR(HALF, CTRL) _Pragma("unroll") for (int k = 0; k < (HALF); ++k) {                          \
      const float t1_ = xs[k] + dpp<CTRL>(xs[k]), t2_ = xs[k + (HALF)] + dpp<CTRL>(xs[k + (HALF)]);  \
      xs[k] = (c & (HALF)) ? t2_ : t1_; }
#define SCORE(rev, nvalid)                                                                           \
  {                                                                                                  \
    TR(8, XOR8) TR(4, XOR7) TR(2, XOR2) TR(1, XOR1)                                                  \
    float t = rbf(rbf(xs[0]) * SCALE) * LOG2E;                     \
    t = (c >> 2) < (nvalid) ? t : -INFINITY;                                                         \
    m = fmaxf(m, t);                                                                                 \
    sl[(rev) * 64] = t;                                                                              \
    SB();                                                                                            \
  }
  const int nrev = (nst + 3) >> 2;
  {
    int left = nst - 4 * (1 + NXR);
    rsrc_t rn = rk;
#pragma nounroll
    for (int rev = NXR; rev < nrev; ++rev) {
      if (rev == nrev - 1) {
        rn = rv; so = so0; left = 4;
        tg = T1 * 64; dt = ((T2 - T1 - (ffn ? AH_FFN2 : cons ? AH_CONS : 0)) * 64) / nst;
      }
#define STEP(g) { const int o_ = left > (g) ? so : lim; QK(g, g) PACE() FILL(rn, g, o_) ADV() }
      STEP(0) STEP(1) STEP(2) STEP(3)
#undef STEP
      SCORE(rev, nst - 4 * rev)
      left -= 4;
#define XREV(e) if constexpr ((e) < NXR) { if (rev == NXR + (e)) {                                   \
        _Pragma("unroll") for (int g = 0; g < 4; ++g) { PIN4(ring + (4 * (1 + (e)) + g) * 4); QK(g, 4 * (1 + (e)) + g) } \
        SCORE(e, 4) } }
      XREV(0) XREV(1)
#undef XREV
    }
  }
  {
    m = xrow_max(row16_max(m));
    float l = 0.f;
#pragma unroll
    for (int rev = 0; rev < NREVMAX; ++rev) {
      const float t = rev < NREVMAX - 3 || rev < nrev ? sl[rev * 64] : -INFINITY;
      const float p = __builtin_amdgcn_exp2f(t - m);
      l += p;
      sl[rev * 64] = p;
    }
    l = wave_sum(l);
    gst(mkrsrc(ws, WS_END), lane == 0 ? 0 : WS_END, WS_ML + (b * NH + h) * 256 + (4 * s + w) * 16, m, l, lds[L_TAG]);
    if (lane == 0) __hip_atomic_fetch_add((LAS unsigned*)(lds + L_MID), 1u, __ATOMIC_RELAXED, __HIP_MEMORY_SCOPE_WORKGROUP);
    SB();
  }
  f32x2 acc[4];
#pragma unroll
  for (int k = 0; k < 4; ++k) acc[k] = f32x2{0.f, 0.f};
#define PV1(g, i) { const float pb = dpp<BCAST + 4 * (g) + (i)>(p); const u32x4 vv = ring[(g) * 4 + (i)];  \
      _Pragma("unroll") for (int k = 0; k < 4; ++k)                                                  \
        acc[k] = __builtin_elementwise_fma(f32x2{pb, pb}, f32x2{blo(vv[k]), bhi(vv[k])}, acc[k]); }
#define PV(g) PV1(g, 0) PV1(g, 1) PV1(g, 2) PV1(g, 3) PIN4(acc);
  int left = nst - 4;
  float pn = sl[0];
#pragma nounroll
  for (int rev = 1; rev < nrev; ++rev) {
    const float p = pn;
    pn = sl[rev * 64];
#define STEP(g) { const int o_ = left > (g) ? so : lim; PV(g) if (left > (g)) PACE() FILL(rv, g, o_) ADV() }
    STEP(0) STEP(1) STEP(2) STEP(3)
#undef STEP
    left -= 4;
  }
  {
    const float p = pn;
    PV(0)
    if (left > -3) { PV(1) }
    if (left > -2) { PV(2) }
    if (left > -1) { PV(3) }
  }
  {
    const float a0 = t32_sum(acc[0][0], acc[2][0]), a1 = t32_sum(acc[0][1], acc[2][1]);
    const float a2 = t32_sum(acc[1][0], acc[3][0]), a3 = t32_sum(acc[1][1], acc[3][1]);
    const float t0 = t16_sum(a0, a2), t1 = t16_sum(a1, a3);
    const unsigned hi = pack2(t0, t1), lo = pack2(t0 - blo(hi), t1 - bhi(hi));
    unsigned* pa = lds + L_A + ((c >> 2) * 64 + (c & 3) * 16 + 2 * w) * 4 + r;
    pa[0] = hi; pa[4] = lo;
  }
  BARRIER();
  if (cons) {
    const int ci = ((bid & 7) >> 1) * 16 + j, cb = ci >> 5, cg = ci & 31;
    const int t = tid - 256, pc = t & 7, hh = t >> 3;
    const unsigned tag = lds[L_TAG];
    const rsrc_t rw = mkrsrc(ws, WS_END);
    const int sy = WS_Y + (cb * 32 + cg) * (128 * 128);
    int vy = hh * 512 + pc * 16;
    int vf = hh < NFW ? WS_F + ((cb * 32 + cg) * NFW + hh) * 128 + pc * 16 : sy + vy;
    int vx = ci == 0 ? WS_ML + NH * 256 + (t & 127) * 64 : vf;
    const unsigned xv = *(const unsigned*)(x + cb * DM + cg * 16 + 2 * pc);
    float k0 = 0.f, k1 = 0.f, g0 = 0.f, g1 = 0.f, a0 = 0.f;
    bool done = false;
    u32x4 ya[5], yb[5];
    unsigned yat, ybt;
#define SWEEP(Y) { _Pragma("unroll") for (int i = 0; i < 4; ++i) Y[i] = gld(rw, vy + i * 128, sy);    \
      Y[4] = gld(rw, vf, 0); Y##t = __builtin_amdgcn_raw_buffer_load_b32(rw, vx, 4, 16); CFENCE(); SB(); }
#define CHECK(Y) {                                                                                   \
      unsigned bad = Y##t ^ tag;                                                                     \
      _Pragma("unroll") for (int i = 0; i < 5; ++i) bad |= (Y[i][1] ^ tag) | (Y[i][3] ^ tag);        \
      const bool take = !done && bad == 0u;                                                          \
      k0 = take ? (__uint_as_float(Y[0][0]) + __uint_as_float(Y[1][0])) + (__uint_as_float(Y[2][0]) + __uint_as_float(Y[3][0])) : k0; \
      k1 = take ? (__uint_as_float(Y[0][2]) + __uint_as_float(Y[1][2])) + (__uint_as_float(Y[2][2]) + __uint_as_float(Y[3][2])) : k1; \
      g0 = take && hh < NFW ? __uint_as_float(Y[4][0]) : g0; g1 = take && hh < NFW ? __uint_as_float(Y[4][2]) : g1; \
      done = done || take;                                                                           \
      vy = done ? WS_END : vy; vf = done ? WS_END : vf; vx = done ? WS_END : vx;                     \
      a0 = t32_sum(k0 + dpp<XOR8>(k0), g0 + dpp<XOR8>(g0));        \
      a0 = t16_sum(a0, t32_sum(k1 + dpp<XOR8>(k1), g1 + dpp<XOR8>(g1)));     \
      if (__builtin_amdgcn_ballot_w64(!done) == 0ull) break;                                         \
      CFENCE(); }
    SWEEP(ya)
    __builtin_amdgcn_s_sleep(12);
    for (unsigned spin = 0; spin < SPIN_CAP; ++spin) { SWEEP(yb) CHECK(ya) SWEEP(ya) CHECK(yb) }
#undef SWEEP
#undef CHECK
    if (c < 8) lds[L_RED + w * 32 + (r >> 1) * 16 + 2 * c + (r & 1)] = __float_as_uint(a0);
    LGKM0();
    unsigned tk = 0;
    if (lane == 0) tk = __hip_atomic_fetch_add((LAS unsigned*)(lds + L_CNT), 16u, __ATOMIC_RELAXED, __HIP_MEMORY_SCOPE_WORKGROUP);
    if (__builtin_amdgcn_readfirstlane(tk) >= 48u) {
      CFENCE();
      if (lane < 8) {
        const float* red = (const float*)lds + L_RED + 2 * lane;
        const f32x2 A = (*(const f32x2*)(red) + *(const f32x2*)(red + 32)) + (*(const f32x2*)(red + 64) + *(const f32x2*)(red + 96));
        const f32x2 F = (*(const f32x2*)(red + 16) + *(const f32x2*)(red + 48)) + (*(const f32x2*)(red + 80) + *(const f32x2*)(red + 112));
        *(unsigned*)(out + cb * DM + cg * 16 + 2 * lane) = pack2(rbf(blo(xv) + rbf(A[0])) + rbf(F[0]), rbf(bhi(xv) + rbf(A[1])) + rbf(F[1]));
      }
      if (ci == 0 && lane == 0) __builtin_amdgcn_raw_buffer_store_b32(tag, rw, WS_E, 0, 16);
    }
    asm volatile("" ::"v"(ya[0]), "v"(ya[1]), "v"(ya[2]), "v"(ya[3]), "v"(ya[4]), "v"(yat), "v"(yb[0]), "v"(yb[1]), "v"(yb[2]), "v"(yb[3]),
                 "v"(yb[4]), "v"(ybt) : "memory");
  }
}

}

extern "C" void kernel_launch(void* const* d_in, const int* in_sizes, int n_in, void* d_out, int out_size, void* d_ws,
                              size_t ws_size, hipStream_t stream) {
  attn_kernel<<<NWG, NTHR, 0, stream>>>((const u16*)d_in[1], (const u16*)d_in[2], (const u16*)d_in[0], (const u16*)d_in[3], (unsigned*)d_ws,
                                        (u16*)d_out, (const u16*)d_in[4], (const u16*)d_in[8], (const u16*)d_in[6], (const u16*)d_in[7]);
}
```

```cpp
#include <hip/hip_runtime.h>
#include <hip/hip_bf16.h>
#include <cstdint>

namespace {

constexpr int NB = 2, DM = 512, NH = 32, SK = 16384, DH = 128, FF = 1792;
constexpr int NSPLIT = 4;
constexpr int NTHR = 512;
constexpr int FSH = 2;
constexpr int NXR = 2;
constexpr int T1 = 3850, T2 = 7600, AH_FFN1 = 300, AH_FFN2 = 100, AH_CONS = 80;
constexpr int WQ_INFL = 8;
constexpr int NWG = NB * NH * NSPLIT;
constexpr int STEPK = 64, STEPB = STEPK * DH * 2;
constexpr int NSTEP = SK / NSPLIT / STEPK;
constexpr int NREVMAX = (NSTEP + FSH + 3) / 4;
constexpr int FSL = 64, NFW = FF / FSL;

constexpr int WS_E = 0;
constexpr int WS_ML = 4096;
constexpr int WS_F = WS_ML + NB * NH * 256;
constexpr int WS_Y = WS_F + NB * 32 * NFW * 128;
constexpr int WS_END = WS_Y + NB * 32 * 128 * 128;
constexpr unsigned SPIN_CAP = 1u << 20;

typedef unsigned u32x4 __attribute__((ext_vector_type(4)));
typedef float f32x4 __attribute__((ext_vector_type(4)));
typedef float f32x2 __attribute__((ext_vector_type(2)));
typedef __bf16 bf16x2 __attribute__((ext_vector_type(2)));
typedef __bf16 bf16x8 __attribute__((ext_vector_type(8)));
typedef unsigned short u16;
#define GAS __attribute__((address_space(1)))
#define LAS __attribute__((address_space(3)))

__device__ __forceinline__ float dot2(unsigned a, unsigned b, float acc) {
  return __builtin_amdgcn_fdot2_f32_bf16(__builtin_bit_cast(bf16x2, a), __builtin_bit_cast(bf16x2, b), acc, false);
}
__device__ __forceinline__ float blo(unsigned u) { return __builtin_bit_cast(float, u << 16); }
__device__ __forceinline__ float bhi(unsigned u) { return __builtin_bit_cast(float, u & 0xffff0000u); }
__device__ __forceinline__ float rbf(float f) { return (float)(__bf16)f; }
__device__ __forceinline__ unsigned pack2(float lo, float hi) {
  bf16x2 v; v.x = (__bf16)lo; v.y = (__bf16)hi; return __builtin_bit_cast(unsigned, v);
}
template <int CTRL> __device__ __forceinline__ float dpp(float x) {
  return __builtin_bit_cast(float, __builtin_amdgcn_mov_dpp(__builtin_bit_cast(int, x), CTRL, 0xf, 0xf, true));
}
constexpr int XOR1 = 0xB1, XOR2 = 0x4E, XOR7 = 0x141, XOR8 = 0x128, BCAST = 0x150;
__device__ __forceinline__ float row16_sum(float x) {
  x += dpp<XOR1>(x); x += dpp<XOR2>(x); x += dpp<XOR7>(x); x += dpp<XOR8>(x); return x;
}
__device__ __forceinline__ float row16_max(float x) {
  x = fmaxf(x, dpp<XOR1>(x)); x = fmaxf(x, dpp<XOR2>(x)); x = fmaxf(x, dpp<XOR7>(x)); return fmaxf(x, dpp<XOR8>(x));
}
__device__ __forceinline__ float t16_sum(float a, float b) {
  auto s = __builtin_amdgcn_permlane16_swap(__float_as_uint(a), __float_as_uint(b), false, false);
  return __uint_as_float(s[0]) + __uint_as_float(s[1]);
}
__device__ __forceinline__ float t32_sum(float a, float b) {
  auto s = __builtin_amdgcn_permlane32_swap(__float_as_uint(a), __float_as_uint(b), false, false);
  return __uint_as_float(s[0]) + __uint_as_float(s[1]);
}
__device__ __forceinline__ float xrow_sum(float x) { x = t16_sum(x, x); return t32_sum(x, x); }
__device__ __forceinline__ float xrow_max(float x) {
  auto s = __builtin_amdgcn_permlane16_swap(__float_as_uint(x), __float_as_uint(x), false, false);
  x = fmaxf(__uint_as_float(s[0]), __uint_as_float(s[1]));
  auto t = __builtin_amdgcn_permlane32_swap(__float_as_uint(x), __float_as_uint(x), false, false);
  return fmaxf(__uint_as_float(t[0]), __uint_as_float(t[1]));
}
__device__ __forceinline__ float wave_sum(float x) { return xrow_sum(row16_sum(x)); }
typedef __amdgpu_buffer_rsrc_t rsrc_t;
__device__ __forceinline__ rsrc_t mkrsrc(const void* p, int bytes) { return __builtin_amdgcn_make_buffer_rsrc((void*)p, 0, bytes, 0x00020000); }
__device__ __forceinline__ u32x4 bld(rsrc_t r, int voff, int soff) { return __builtin_amdgcn_raw_buffer_load_b128(r, voff, soff, 2); }
__device__ __forceinline__ u32x4 bldc(rsrc_t r, int voff, int soff) { return __builtin_amdgcn_raw_buffer_load_b128(r, voff, soff, 0); }
__device__ __forceinline__ u32x4 gld(rsrc_t r, int voff, int soff) { return __builtin_amdgcn_raw_buffer_load_b128(r, voff, soff, 16); }
__device__ __forceinline__ void gst(rsrc_t r, int voff, int soff, float v0, float v1, unsigned tag) {
  __builtin_amdgcn_raw_buffer_store_b128(u32x4{__float_as_uint(v0), tag, __float_as_uint(v1), tag}, r, voff, soff, 16);
}
#define SB() __builtin_amdgcn_sched_barrier(0)
__device__ __forceinline__ unsigned load_tag(const unsigned* ws) {
  unsigned e;
  asm volatile("s_load_dword %0, %1, 0x0\n\ts_waitcnt lgkmcnt(0)" : "=s"(e) : "s"(ws) : "memory");
  return e + 1u;
}
#define PIN4(a) asm volatile("" : "+v"((a)[0]), "+v"((a)[1]), "+v"((a)[2]), "+v"((a)[3])::"memory"); SB()
#define LGKM0() asm volatile("s_waitcnt lgkmcnt(0)" ::: "memory")
#define CFENCE() asm volatile("" ::: "memory")
#define BARRIER() do { LGKM0(); __builtin_amdgcn_s_barrier(); CFENCE(); } while (0)
__device__ __forceinline__ f32x4 mfma(u32x4 a, u32x4 b, f32x4 c) {
  return __builtin_amdgcn_mfma_f32_16x16x32_bf16(__builtin_bit_cast(bf16x8, a), __builtin_bit_cast(bf16x8, b), c, 0, 0, 0);
}
__device__ __forceinline__ u32x4 bfrag(const u32x4* w, int e) {
  u32x4 o;
#pragma unroll
  for (int jj = 0; jj < 4; ++jj) o[jj] = __builtin_amdgcn_perm(w[2 * jj + 1][e >> 1], w[2 * jj][e >> 1], (e & 1) ? 0x07060302u : 0x05040100u);
  return o;
}
__device__ __forceinline__ u32x4 layernorm8(u32x4 xa) {
  float e[8];
#pragma unroll
  for (int k = 0; k < 4; ++k) { e[2 * k] = blo(xa[k]); e[2 * k + 1] = bhi(xa[k]); }
  const float mean = wave_sum(((e[0] + e[1]) + (e[2] + e[3])) + ((e[4] + e[5]) + (e[6] + e[7]))) * (1.0f / DM);
  float q = 0.f;
#pragma unroll
  for (int k = 0; k < 8; ++k) { const float d = e[k] - mean; q = fmaf(d, d, q); }
  const float var = wave_sum(q) * (1.0f / DM);
  const float mb = rbf(mean), sd = rbf(sqrtf(rbf(rbf(var) + 1e-5f)));
  const float ri = 1.0f / sd;
  u32x4 o;
#pragma unroll
  for (int k = 0; k < 4; ++k) {
    const float d0 = rbf(e[2 * k] - mb), d1 = rbf(e[2 * k + 1] - mb);
    float q0 = d0 * ri, q1 = d1 * ri;
    q0 = fmaf(fmaf(-q0, sd, d0), ri, q0); q1 = fmaf(fmaf(-q1, sd, d1), ri, q1);
    o[k] = pack2(q0, q1);
  }
  return o;
}

constexpr int L_QP = 0;
constexpr int L_A = L_QP + 4 * DH;
constexpr int L_CNT = L_A + 1024, L_TAG = L_CNT + 1, L_MID = L_CNT + 2, L_FW = L_CNT + 4;
constexpr int L_RED = L_CNT + 16;
constexpr int L_XN = L_RED + 128;
constexpr int L_GU = L_XN + 4 * 128;
constexpr int L_H = L_GU + 4 * 256;
constexpr int L_S = L_H + 4 * 64;
constexpr int L_TOT = L_S + 4 * NREVMAX * 64;

__global__ void __launch_bounds__(NTHR, 2) attn_kernel(const u16* __restrict__ kc, const u16* __restrict__ vc, const u16* __restrict__ x,
                                                       const u16* __restrict__ wq, unsigned* ws, u16* __restrict__ out,
                                                       const u16* __restrict__ wo, const u16* __restrict__ w2,
                                                       const u16* __restrict__ w1, const u16* __restrict__ w3) {
  __shared__ __attribute__((aligned(16))) unsigned lds[L_TOT];
  const int bid = blockIdx.x;
  const int tid = threadIdx.x, lane = tid & 63, wv = __builtin_amdgcn_readfirstlane(tid >> 6);
  const int j = bid >> 3, h = (bid & 7) * 4 + (j >> 3), b = (j >> 2) & 1, s = j & 3;
  const int r = lane >> 4, c = lane & 15;
  const bool fgrp = (bid & 1) == 0 && (j >> 2) < 7;
  const int w = wv - 4;
  const int fs = fgrp ? FSH : 0;
  int nst = NSTEP + (s == 0 ? -3 * fs : fs);
  int kvb = ((b * NH + h) * NSPLIT * NSTEP + s * NSTEP + (s == 0 ? 0 : (s - 4) * fs)) * STEPB;
  int so0 = w * 4096 + ((2 * s + b) * 8 + (s == 0 ? 0 : (4 - s) * fs)) * STEPB;
  asm volatile("" : "+s"(nst), "+s"(kvb), "+s"(so0) : "s"(kc), "s"(vc), "s"(x), "s"(wq), "s"(ws), "s"(out));

  if (wv < 4) {
    const int a = wv;
    __builtin_amdgcn_s_setprio(3);
    const rsrc_t rx = mkrsrc(x, NB * DM * 2);
    const u32x4 xa = bldc(rx, lane * 16, b * (DM * 2));
    u32x4 wr[32];
    const rsrc_t rq = mkrsrc(wq + h * DH, (DM * NH * DH - h * DH) * 2);
    const int vq = r * (8 * NH * DH * 2) + c * 16;
#define LD_WQ(i) { if ((i) >= WQ_INFL) asm volatile("s_waitcnt vmcnt(%0)" ::"n"(WQ_INFL - 1) : "memory"); \
      wr[i] = bldc(rq, vq, (128 * a + 32 * ((((i) >> 3) + s) & 3) + ((i) & 7)) * (NH * DH * 2)); SB(); }
#pragma unroll
    for (int i = 0; i < WQ_INFL; ++i) LD_WQ(i)
    if (a == 0) {
      const unsigned t = load_tag(ws);
      if (lane == 0) { lds[L_CNT] = 0u; lds[L_TAG] = t; lds[L_MID] = 0u; }
    }
    SB();
    unsigned* xn = lds + L_XN + a * 128;
    {
      const u32x4 n0 = layernorm8(xa);
      if (r == a) *(u32x4*)(xn + 64 * b + 4 * c) = n0;
    }
    CFENCE(); SB();
    f32x4 d[8];
#pragma unroll
    for (int e = 0; e < 8; ++e) d[e] = f32x4{0.f, 0.f, 0.f, 0.f};
#pragma unroll
    for (int ks = 0; ks < 4; ++ks) {
#pragma unroll
      for (int i = 8 * ks + WQ_INFL; i < 8 * ks + 8 + WQ_INFL; ++i) if (i < 32) LD_WQ(i)
      const u32x4 af = *(const u32x4*)(xn + 64 * b + 16 * ((ks + s) & 3) + 4 * r);
#pragma unroll
      for (int e = 0; e < 8; ++e) d[e] = mfma(af, bfrag(wr + 8 * ks, e), d[e]);
      SB();
    }
#undef LD_WQ
    if (r == 0) {
      *(f32x4*)(lds + L_QP + a * DH + 8 * c) = f32x4{d[0][0], d[1][0], d[2][0], d[3][0]};
      *(f32x4*)(lds + L_QP + a * DH + 8 * c + 4) = f32x4{d[4][0], d[5][0], d[6][0], d[7][0]};
    }
    BARRIER();
    const unsigned tag = lds[L_TAG];
    const rsrc_t rw = mkrsrc(ws, WS_END);
    u32x4 bf[32];
    {
      const rsrc_t ro = mkrsrc(wo + (size_t)h * DH * DM, DH * DM * 2);
#pragma unroll
      for (int ks = 0; ks < 4; ++ks) {
#pragma unroll
        for (int i = 0; i < 8; ++i) {
          __builtin_amdgcn_s_sleep(8);
          wr[i] = bldc(ro, r * 8192 + c * 16 + (i & 3) * 1024, ks * 32768 + (i >> 2) * 4096 + a * 256);
          SB();
        }
#pragma unroll
        for (int e = 0; e < 8; ++e) { bf[8 * ks + e] = bfrag(wr, e); asm volatile("" : "+v"(bf[8 * ks + e])); }
        SB();
      }
    }
    if (fgrp && s == 0) {
      const int sl = ((bid & 7) >> 1) * 7 + (j >> 2), f0 = sl * FSL;
      const u32x4 xo = bldc(rx, lane * 16, (b ^ 1) * (DM * 2));
      const GAS u32x4* p13 = (const GAS u32x4*)((c < 8 ? w1 : w3) + (size_t)(128 * a + 8 * r) * FF + f0 + 8 * (c & 7));
#pragma unroll
      for (int i = 0; i < 8; ++i) wr[i] = __builtin_nontemporal_load(p13 + i * (FF / 8));
      SB();
      {
        const u32x4 n1 = layernorm8(xo);
        if (r == a) *(u32x4*)(xn + 64 * (b ^ 1) + 4 * c) = n1;
      }
      CFENCE();
#pragma unroll
      for (int e = 0; e < 8; ++e) d[e] = f32x4{0.f, 0.f, 0.f, 0.f};
      const rsrc_t r2 = mkrsrc(w2 + (size_t)f0 * DM, FSL * DM * 2);
#pragma unroll
      for (int ks = 0; ks < 4; ++ks) {
        const u32x4 af = *(const u32x4*)(xn + 64 * (c & 1) + 16 * ks + 4 * r);
#pragma unroll
        for (int e = 0; e < 8; ++e) d[e] = mfma(af, bfrag(wr, e), d[e]);
        SB();
#pragma unroll
        for (int jj = 0; jj < 8; ++jj)
          wr[jj] = ks < 3 ? __builtin_nontemporal_load(p13 + (32 * (ks + 1) + jj) * (FF / 8))
                          : bld(r2, r * 8192 + c * 16 + (jj & 3) * 1024, (jj >> 2) * 4096 + a * 256);
        SB();
      }
      if (r < 2) {
        float* gu = (float*)lds + L_GU + a * 256 + r * 128 + 8 * c;
        *(f32x4*)(gu) = r ? f32x4{d[0][1], d[1][1], d[2][1], d[3][1]} : f32x4{d[0][0], d[1][0], d[2][0], d[3][0]};
        *(f32x4*)(gu + 4) = r ? f32x4{d[4][1], d[5][1], d[6][1], d[7][1]} : f32x4{d[4][0], d[5][0], d[6][0], d[7][0]};
      }
      LGKM0();
      if (lane == 0) __hip_atomic_fetch_add((LAS unsigned*)(lds + L_CNT), 1u, __ATOMIC_RELAXED, __HIP_MEMORY_SCOPE_WORKGROUP);
      while (*(volatile LAS unsigned*)(lds + L_CNT) < 4u) __builtin_amdgcn_s_sleep(1);
      CFENCE();
      {
        const float* gu = (const float*)lds + L_GU + (lane >> 5) * 128 + 2 * (lane & 31);
        f32x2 g = {0.f, 0.f}, u = {0.f, 0.f};
#pragma unroll
        for (int aa = 0; aa < 4; ++aa) { g += *(const f32x2*)(gu + aa * 256); u += *(const f32x2*)(gu + aa * 256 + 64); }
        float hv[2];
#pragma unroll
        for (int k = 0; k < 2; ++k) { const float gg = rbf(g[k]); hv[k] = rbf(gg / (1.0f + __expf(-gg))) * rbf(u[k]); }
        lds[L_H + a * 64 + lane] = pack2(hv[0], hv[1]);
      }
      CFENCE();
#pragma unroll
      for (int e = 0; e < 8; ++e) d[e] = f32x4{0.f, 0.f, 0.f, 0.f};
#pragma unroll
      for (int ks = 0; ks < 2; ++ks) {
        const u32x4 af = *(const u32x4*)(lds + L_H + a * 64 + 32 * (c & 1) + 16 * ks + 4 * r);
#pragma unroll
        for (int e = 0; e < 8; ++e) d[e] = mfma(af, bfrag(wr, e), d[e]);
        SB();
        if (ks == 0) {
#pragma unroll
          for (int jj = 0; jj < 8; ++jj) wr[jj] = bld(r2, r * 8192 + c * 16 + (jj & 3) * 1024, 32768 + (jj >> 2) * 4096 + a * 256);
          SB();
        }
      }
      {
        const int fo = WS_F + (((r & 1) * 32 + 8 * a + (c >> 1)) * NFW + sl) * 128 + (c & 1) * 64 + (r >> 1) * 32;
        float t[4];
#pragma unroll
        for (int e = 0; e < 4; ++e) {
          const float p0 = (r & 1) ? d[e][1] : d[e][0], p1 = (r & 1) ? d[4 + e][1] : d[4 + e][0];
          t[e] = r < 2 ? p0 : p1;
        }
        gst(rw, fo, 0, t[0], t[1], tag); gst(rw, fo + 16, 0, t[2], t[3], tag);
      }
      SB();
    }
    if (a == 0) {
      while (*(volatile LAS unsigned*)(lds + L_MID) < 4u) __builtin_amdgcn_s_sleep(16);
      if (!(fgrp && s == 0)) for (int i = 0; i < 3; ++i) __builtin_amdgcn_s_sleep(127);
      u32x4 ml;
      for (unsigned spin = 0; spin < SPIN_CAP; ++spin) {
        CFENCE();
        ml = gld(rw, c * 16, WS_ML + (b * NH + h) * 256);
        if (__builtin_amdgcn_ballot_w64(((ml[1] ^ tag) | (ml[3] ^ tag)) != 0u) == 0ull) break;
        __builtin_amdgcn_s_sleep(64);
      }
      const float mw = __uint_as_float(ml[0]), M = row16_max(mw);
      const float e = __builtin_amdgcn_exp2f(mw - M);
      const float f = e / row16_sum(e * __uint_as_float(ml[2]));
      if ((lane >> 2) == s) lds[L_FW + (lane & 3)] = __float_as_uint(f);
    }
    BARRIER();
    u32x4 af[4];
#pragma unroll
    for (int ks = 0; ks < 4; ++ks) af[ks] = *(const u32x4*)(lds + L_A + (ks * 64 + r * 16 + (c & 7)) * 4);
    const f32x2 fw = *(const f32x2*)(lds + L_FW + 2 * (r & 1));
    const float f0 = fw[0], f1 = fw[1];
#pragma unroll
    for (int e = 0; e < 8; ++e) d[e] = f32x4{0.f, 0.f, 0.f, 0.f};
#pragma unroll
    for (int ks = 0; ks < 4; ++ks)
#pragma unroll
      for (int e = 0; e < 8; ++e) d[e] = mfma(af[ks], bf[8 * ks + e], d[e]);
    float v[4];
#pragma unroll
    for (int e = 0; e < 4; ++e) {
      const float p0 = d[e][0] + d[e][1], p1 = d[e][2] + d[e][3], q0 = d[4 + e][0] + d[4 + e][1], q1 = d[4 + e][2] + d[4 + e][3];
      v[e] = f0 * (r < 2 ? p0 : q0) + f1 * (r < 2 ? p1 : q1);
    }
    gst(rw, (c >> 1) * (128 * 128) + (c & 1) * 64 + r * 16, WS_Y + ((b * 32 + 8 * a) * 128 + h * NSPLIT + s) * 128,
        t16_sum(v[0], v[2]), t16_sum(v[1], v[3]), tag);
    return;
  }

  u32x4 ring[16 * (1 + NXR)];
  const int vo = lane * 16;
  const int lim = nst * STEPB;
  const rsrc_t rk = mkrsrc((const char*)kc + (unsigned)kvb, lim), rv = mkrsrc((const char*)vc + (unsigned)kvb, lim);
  int so = so0;
  const bool ffn = fgrp && s == 0, cons = (bid & 1) == 0 && j < 16;
  const int tstart = (int)__builtin_amdgcn_s_memrealtime();
  constexpr int NF = NSTEP - 3 * FSH, NS = NSTEP + FSH;
  int dt = ffn ? T1 * 64 / NF : fgrp ? T1 * 64 / NS : T1 * 64 / NSTEP;
  int tg = dt * 4 * (1 + NXR) - (ffn ? AH_FFN1 * 64 : 0);
#define PACE() { while (((int)__builtin_amdgcn_s_memrealtime() - tstart) * 64 < tg) __builtin_amdgcn_s_sleep(1); tg += dt; }
#define ADV() so += STEPB; so = so >= lim ? so - lim : so;
#define FILL(rs, g, o) _Pragma("unroll") for (int i = 0; i < 4; ++i) { ring[(g) * 4 + i] = bld(rs, vo + i * 1024, o); SB(); }
#pragma unroll
  for (int g = 4; g < 4 + 4 * NXR; ++g) { FILL(rk, g, so) ADV() }
#pragma unroll
  for (int g = 0; g < 4; ++g) { FILL(rk, g, so) ADV() }
  BARRIER();
  unsigned qp[4];
  {
    f32x4 t0 = *(const f32x4*)(lds + L_QP + 8 * c), t1 = *(const f32x4*)(lds + L_QP + 8 * c + 4);
#pragma unroll
    for (int aa = 1; aa < 4; ++aa) {
      t0 += *(const f32x4*)(lds + L_QP + aa * DH + 8 * c);
      t1 += *(const f32x4*)(lds + L_QP + aa * DH + 8 * c + 4);
    }
    qp[0] = pack2(t0[0], t0[1]); qp[1] = pack2(t0[2], t0[3]); qp[2] = pack2(t1[0], t1[1]); qp[3] = pack2(t1[2], t1[3]);
  }
  constexpr float SCALE = 0.088388347648318440f, LOG2E = 1.4426950408889634f;
  float* sl = (float*)lds + L_S + w * (NREVMAX * 64) + lane;
  float m = -INFINITY;
  float xs[16];
#define QK(g, slot)                                                                                  \
    _Pragma("unroll") for (int i = 0; i < 4; ++i) {                                                  \
      float d_ = 0.f;                                                                                \
      _Pragma("unroll") for (int k = 0; k < 4; ++k) d_ = dot2(ring[(slot) * 4 + i][k], qp[k], d_);   \
      xs[(g) * 4 + i] = d_;                                                                          \
    }                                                                                                \
    PIN4(xs + (g) * 4);
#define TR(HALF, CTRL) _Pragma("unroll") for (int k = 0; k < (HALF); ++k) {                          \
      const float t1_ = xs[k] + dpp<CTRL>(xs[k]), t2_ = xs[k + (HALF)] + dpp<CTRL>(xs[k + (HALF)]);  \
      xs[k] = (c & (HALF)) ? t2_ : t1_; }
#define SCORE(rev, nvalid)                                                                           \
  {                                                                                                  \
    TR(8, XOR8) TR(4, XOR7) TR(2, XOR2) TR(1, XOR1)                                                  \
    float t = rbf(rbf(xs[0]) * SCALE) * LOG2E;                     \
    t = (c >> 2) < (nvalid) ? t : -INFINITY;                                                         \
    m = fmaxf(m, t);                                                                                 \
    sl[(rev) * 64] = t;                                                                              \
    SB();                                                                                            \
  }
  const int nrev = (nst + 3) >> 2;
  {
    int left = nst - 4 * (1 + NXR);
    rsrc_t rn = rk;
#pragma nounroll
    for (int rev = NXR; rev < nrev; ++rev) {
      if (rev == nrev - 1) {
        rn = rv; so = so0; left = 4;
        tg = T1 * 64;
        dt = ffn ? (T2 - T1 - AH_FFN2) * 64 / NF : cons ? (T2 - T1 - AH_CONS) * 64 / NS : fgrp ? (T2 - T1) * 64 / NS : (T2 - T1) * 64 / NSTEP;
      }
#define STEP(g) { const int o_ = left > (g) ? so : lim; QK(g, g) PACE() FILL(rn, g, o_) ADV() }
      STEP(0) STEP(1) STEP(2) STEP(3)
#undef STEP
      SCORE(rev, nst - 4 * rev)
      left -= 4;
#define XREV(e) if constexpr ((e) < NXR) { if (rev == NXR + (e)) {                                   \
        _Pragma("unroll") for (int g = 0; g < 4; ++g) { PIN4(ring + (4 * (1 + (e)) + g) * 4); QK(g, 4 * (1 + (e)) + g) } \
        SCORE(e, 4) } }
      XREV(0) XREV(1)
#undef XREV
    }
  }
  {
    m = xrow_max(row16_max(m));
    float l = 0.f;
#pragma unroll
    for (int rev = 0; rev < NREVMAX; ++rev) {
      const float t = rev < NREVMAX - 3 || rev < nrev ? sl[rev * 64] : -INFINITY;
      const float p = __builtin_amdgcn_exp2f(t - m);
      l += p;
      sl[rev * 64] = p;
    }
    l = wave_sum(l);
    gst(mkrsrc(ws, WS_END), lane == 0 ? 0 : WS_END, WS_ML + (b * NH + h) * 256 + (4 * s + w) * 16, m, l, lds[L_TAG]);
    if (lane == 0) __hip_atomic_fetch_add((LAS unsigned*)(lds + L_MID), 1u, __ATOMIC_RELAXED, __HIP_MEMORY_SCOPE_WORKGROUP);
    SB();
  }
  f32x2 acc[4];
#pragma unroll
  for (int k = 0; k < 4; ++k) acc[k] = f32x2{0.f, 0.f};
#define PV1(g, i) { const float pb = dpp<BCAST + 4 * (g) + (i)>(p); const u32x4 vv = ring[(g) * 4 + (i)];  \
      _Pragma("unroll") for (int k = 0; k < 4; ++k)                                                  \
        acc[k] = __builtin_elementwise_fma(f32x2{pb, pb}, f32x2{blo(vv[k]), bhi(vv[k])}, acc[k]); }
#define PV(g) PV1(g, 0) PV1(g, 1) PV1(g, 2) PV1(g, 3) PIN4(acc);
  int left = nst - 4;
  float pn = sl[0];
#pragma nounroll
  for (int rev = 1; rev < nrev; ++rev) {
    const float p = pn;
    pn = sl[rev * 64];
#define STEP(g) { const int o_ = left > (g) ? so : lim; PV(g) if (left > (g)) PACE() FILL(rv, g, o_) ADV() }
    STEP(0) STEP(1) STEP(2) STEP(3)
#undef STEP
    left -= 4;
  }
  {
    const float p = pn;
    PV(0)
    if (left > -3) { PV(1) }
    if (left > -2) { PV(2) }
    if (left > -1) { PV(3) }
  }
  {
    const float a0 = t32_sum(acc[0][0], acc[2][0]), a1 = t32_sum(acc[0][1], acc[2][1]);
    const float a2 = t32_sum(acc[1][0], acc[3][0]), a3 = t32_sum(acc[1][1], acc[3][1]);
    const float t0 = t16_sum(a0, a2), t1 = t16_sum(a1, a3);
    const unsigned hi = pack2(t0, t1), lo = pack2(t0 - blo(hi), t1 - bhi(hi));
    unsigned* pa = lds + L_A + ((c >> 2) * 64 + (c & 3) * 16 + 2 * w) * 4 + r;
    pa[0] = hi; pa[4] = lo;
  }
  BARRIER();
  if (cons) {
    const int ci = ((bid & 7) >> 1) * 16 + j, cb = ci >> 5, cg = ci & 31;
    const int t = tid - 256, pc = t & 7, hh = t >> 3;
    const unsigned tag = lds[L_TAG];
    const rsrc_t rw = mkrsrc(ws, WS_END);
    const int sy = WS_Y + (cb * 32 + cg) * (128 * 128);
    int vy = hh * 512 + pc * 16;
    int vf = hh < NFW ? WS_F + ((cb * 32 + cg) * NFW + hh) * 128 + pc * 16 : sy + vy;
    int vx = ci == 0 ? WS_ML + NH * 256 + (t & 127) * 64 : vf;
    const unsigned xv = *(const unsigned*)(x + cb * DM + cg * 16 + 2 * pc);
    float k0 = 0.f, k1 = 0.f, g0 = 0.f, g1 = 0.f, a0 = 0.f;
    bool done = false;
    u32x4 ya[5], yb[5];
    unsigned yat, ybt;
#define SWEEP(Y) { _Pragma("unroll") for (int i = 0; i < 4; ++i) Y[i] = gld(rw, vy + i * 128, sy);    \
      Y[4] = gld(rw, vf, 0); Y##t = __builtin_amdgcn_raw_buffer_load_b32(rw, vx, 4, 16); CFENCE(); SB(); }
#define CHECK(Y) {                                                                                   \
      unsigned bad = Y##t ^ tag;                                                                     \
      _Pragma("unroll") for (int i = 0; i < 5; ++i) bad |= (Y[i][1] ^ tag) | (Y[i][3] ^ tag);        \
      const bool take = !done && bad == 0u;                                                          \
      k0 = take ? (__uint_as_float(Y[0][0]) + __uint_as_float(Y[1][0])) + (__uint_as_float(Y[2][0]) + __uint_as_float(Y[3][0])) : k0; \
      k1 = take ? (__uint_as_float(Y[0][2]) + __uint_as_float(Y[1][2])) + (__uint_as_float(Y[2][2]) + __uint_as_float(Y[3][2])) : k1; \
      g0 = take && hh < NFW ? __uint_as_float(Y[4][0]) : g0; g1 = take && hh < NFW ? __uint_as_float(Y[4][2]) : g1; \
      done = done || take;                                                                           \
      vy = done ? WS_END : vy; vf = done ? WS_END : vf; vx = done ? WS_END : vx;                     \
      a0 = t32_sum(k0 + dpp<XOR8>(k0), g0 + dpp<XOR8>(g0));        \
      a0 = t16_sum(a0, t32_sum(k1 + dpp<XOR8>(k1), g1 + dpp<XOR8>(g1)));     \
      if (__builtin_amdgcn_ballot_w64(!done) == 0ull) break;                                         \
      CFENCE(); }
    SWEEP(ya)
    __builtin_amdgcn_s_sleep(12);
    for (unsigned spin = 0; spin < SPIN_CAP; ++spin) { SWEEP(yb) CHECK(ya) SWEEP(ya) CHECK(yb) }
#undef SWEEP
#undef CHECK
    if (c < 8) lds[L_RED + w * 32 + (r >> 1) * 16 + 2 * c + (r & 1)] = __float_as_uint(a0);
    LGKM0();
    unsigned tk = 0;
    if (lane == 0) tk = __hip_atomic_fetch_add((LAS unsigned*)(lds + L_CNT), 16u, __ATOMIC_RELAXED, __HIP_MEMORY_SCOPE_WORKGROUP);
    if (__builtin_amdgcn_readfirstlane(tk) >= 48u) {
      CFENCE();
      if (lane < 8) {
        const float* red = (const float*)lds + L_RED + 2 * lane;
        const f32x2 A = (*(const f32x2*)(red) + *(const f32x2*)(red + 32)) + (*(const f32x2*)(red + 64) + *(const f32x2*)(red + 96));
        const f32x2 F = (*(const f32x2*)(red + 16) + *(const f32x2*)(red + 48)) + (*(const f32x2*)(red + 80) + *(const f32x2*)(red + 112));
        *(unsigned*)(out + cb * DM + cg * 16 + 2 * lane) = pack2(rbf(blo(xv) + rbf(A[0])) + rbf(F[0]), rbf(bhi(xv) + rbf(A[1])) + rbf(F[1]));
      }
      if (ci == 0 && lane == 0) __builtin_amdgcn_raw_buffer_store_b32(tag, rw, WS_E, 0, 16);
    }
    asm volatile("" ::"v"(ya[0]), "v"(ya[1]), "v"(ya[2]), "v"(ya[3]), "v"(ya[4]), "v"(yat), "v"(yb[0]), "v"(yb[1]), "v"(yb[2]), "v"(yb[3]),
                 "v"(yb[4]), "v"(ybt) : "memory");
  }
}

}

extern "C" void kernel_launch(void* const* d_in, const int* in_sizes, int n_in, void* d_out, int out_size, void* d_ws,
                              size_t ws_size, hipStream_t stream) {
  attn_kernel<<<NWG, NTHR, 0, stream>>>((const u16*)d_in[1], (const u16*)d_in[2], (const u16*)d_in[0], (const u16*)d_in[3], (unsigned*)d_ws,
                                        (u16*)d_out, (const u16*)d_in[4], (const u16*)d_in[8], (const u16*)d_in[6], (const u16*)d_in[7]);
}
```

```cpp
#include <hip/hip_runtime.h>
#include <hip/hip_bf16.h>
#include <cstdint>

#define WQ_INFL 8
#define WO_SLEEP 8
#define CONS_PAR 0
#define POLL_MANY 64
#define POLL_SLEEP 16
#define OAUX 0
#define PHASE_FIX 1
#define ABL 0
#define ML_SLEEP 3
#define PIPE 1
#define SMUL 1
#define T1 3775
#define T2 7500
#define PSL 1
#define PGR 1
#define FFN_AHEAD 300
#define FFN_AHEAD2 100
#define CONS_AHEAD 80
#define EVEN_AHEAD 0
#define PB 0
#define LEAD 0
#define SPRINT 0
#define FFN_SLEEP 0
#define FSLEEP() if (FFN_SLEEP) __builtin_amdgcn_s_sleep(FFN_SLEEP)
#define DRY 0
#define CAPN 0
#define CAPB 30
#define WSTAG 0
#define JSTAG 0
#define P1FIX 1
#define PAT 1
#define CBASE 1
#define FSH 2
#define NXR 2

namespace {

constexpr int NB = 2, DM = 512, NH = 32, SK = 16384, DH = 128, FF = 1792;
constexpr int NSPLIT = 4;
constexpr int NTHR = 512;
constexpr int NWG = NB * NH * NSPLIT;
constexpr int STEPK = 64, STEPB = STEPK * DH * 2;
constexpr int NSTEP = SK / NSPLIT / STEPK;
constexpr int NREVMAX = (NSTEP + FSH + 3) / 4;
constexpr int FSL = 64, NFW = FF / FSL;

constexpr int WS_E = 0;
constexpr int WS_ML = 4096;
constexpr int WS_F = WS_ML + NB * NH * 256;
constexpr int WS_Y = WS_F + NB * 32 * NFW * 128;
constexpr int WS_END = WS_Y + NB * 32 * 128 * 128;
constexpr unsigned SPIN_CAP = 1u << 20;

typedef unsigned u32x4 __attribute__((ext_vector_type(4)));
typedef float f32x4 __attribute__((ext_vector_type(4)));
typedef float f32x2 __attribute__((ext_vector_type(2)));
typedef __bf16 bf16x2 __attribute__((ext_vector_type(2)));
typedef __bf16 bf16x8 __attribute__((ext_vector_type(8)));
typedef unsigned short u16;
#define GAS __attribute__((address_space(1)))
#define LAS __attribute__((address_space(3)))

__device__ __forceinline__ float dot2(unsigned a, unsigned b, float acc) {
  return __builtin_amdgcn_fdot2_f32_bf16(__builtin_bit_cast(bf16x2, a), __builtin_bit_cast(bf16x2, b), acc, false);
}
__device__ __forceinline__ float blo(unsigned u) { return __builtin_bit_cast(float, u << 16); }
__device__ __forceinline__ float bhi(unsigned u) { return __builtin_bit_cast(float, u & 0xffff0000u); }
__device__ __forceinline__ float rbf(float f) { return (float)(__bf16)f; }
__device__ __forceinline__ unsigned pack2(float lo, float hi) {
  bf16x2 v; v.x = (__bf16)lo; v.y = (__bf16)hi; return __builtin_bit_cast(unsigned, v);
}
template <int CTRL> __device__ __forceinline__ float dpp(float x) {
  return __builtin_bit_cast(float, __builtin_amdgcn_mov_dpp(__builtin_bit_cast(int, x), CTRL, 0xf, 0xf, true));
}
constexpr int XOR1 = 0xB1, XOR2 = 0x4E, XOR7 = 0x141, XOR8 = 0x128, BCAST = 0x150;
__device__ __forceinline__ float row16_sum(float x) {
  x += dpp<XOR1>(x); x += dpp<XOR2>(x); x += dpp<XOR7>(x); x += dpp<XOR8>(x); return x;
}
__device__ __forceinline__ float row16_max(float x) {
  x = fmaxf(x, dpp<XOR1>(x)); x = fmaxf(x, dpp<XOR2>(x)); x = fmaxf(x, dpp<XOR7>(x)); return fmaxf(x, dpp<XOR8>(x));
}
__device__ __forceinline__ float t16_sum(float a, float b) {
  auto s = __builtin_amdgcn_permlane16_swap(__float_as_uint(a), __float_as_uint(b), false, false);
  return __uint_as_float(s[0]) + __uint_as_float(s[1]);
}
__device__ __forceinline__ float t32_sum(float a, float b) {
  auto s = __builtin_amdgcn_permlane32_swap(__float_as_uint(a), __float_as_uint(b), false, false);
  return __uint_as_float(s[0]) + __uint_as_float(s[1]);
}
__device__ __forceinline__ float xrow_sum(float x) { x = t16_sum(x, x); return t32_sum(x, x); }
__device__ __forceinline__ float xrow_max(float x) {
  auto s = __builtin_amdgcn_permlane16_swap(__float_as_uint(x), __float_as_uint(x), false, false);
  x = fmaxf(__uint_as_float(s[0]), __uint_as_float(s[1]));
  auto t = __builtin_amdgcn_permlane32_swap(__float_as_uint(x), __float_as_uint(x), false, false);
  return fmaxf(__uint_as_float(t[0]), __uint_as_float(t[1]));
}
__device__ __forceinline__ float wave_sum(float x) { return xrow_sum(row16_sum(x)); }
typedef __amdgpu_buffer_rsrc_t rsrc_t;
__device__ __forceinline__ rsrc_t mkrsrc(const void* p, int bytes) { return __builtin_amdgcn_make_buffer_rsrc((void*)p, 0, bytes, 0x00020000); }
__device__ __forceinline__ u32x4 bld(rsrc_t r, int voff, int soff) { return __builtin_amdgcn_raw_buffer_load_b128(r, voff, soff, 2); }
__device__ __forceinline__ u32x4 bldc(rsrc_t r, int voff, int soff) { return __builtin_amdgcn_raw_buffer_load_b128(r, voff, soff, 0); }
__device__ __forceinline__ u32x4 gld(rsrc_t r, int voff, int soff) { return __builtin_amdgcn_raw_buffer_load_b128(r, voff, soff, 16); }
__device__ __forceinline__ void gst(rsrc_t r, int voff, int soff, float v0, float v1, unsigned tag) {
  __builtin_amdgcn_raw_buffer_store_b128(u32x4{__float_as_uint(v0), tag, __float_as_uint(v1), tag}, r, voff, soff, 16);
}
#define SB() __builtin_amdgcn_sched_barrier(0)
__device__ __forceinline__ unsigned load_tag(const unsigned* ws) {
  unsigned e;
  asm volatile("s_load_dword %0, %1, 0x0\n\ts_waitcnt lgkmcnt(0)" : "=s"(e) : "s"(ws) : "memory");
  return e + 1u;
}
#define PIN4(a) asm volatile("" : "+v"((a)[0]), "+v"((a)[1]), "+v"((a)[2]), "+v"((a)[3])::"memory"); SB()
#define LGKM0() asm volatile("s_waitcnt lgkmcnt(0)" ::: "memory")
__device__ __forceinline__ f32x4 mfma(u32x4 a, u32x4 b, f32x4 c) {
  return __builtin_amdgcn_mfma_f32_16x16x32_bf16(__builtin_bit_cast(bf16x8, a), __builtin_bit_cast(bf16x8, b), c, 0, 0, 0);
}
__device__ __forceinline__ u32x4 bfrag(const u32x4* w, int e) {
  u32x4 o;
#pragma unroll
  for (int jj = 0; jj < 4; ++jj) o[jj] = __builtin_amdgcn_perm(w[2 * jj + 1][e >> 1], w[2 * jj][e >> 1], (e & 1) ? 0x07060302u : 0x05040100u);
  return o;
}
__device__ __forceinline__ u32x4 layernorm8(u32x4 xa) {
  float e[8];
#pragma unroll
  for (int k = 0; k < 4; ++k) { e[2 * k] = blo(xa[k]); e[2 * k + 1] = bhi(xa[k]); }
  const float mean = wave_sum(((e[0] + e[1]) + (e[2] + e[3])) + ((e[4] + e[5]) + (e[6] + e[7]))) * (1.0f / DM);
  float q = 0.f;
#pragma unroll
  for (int k = 0; k < 8; ++k) { const float d = e[k] - mean; q = fmaf(d, d, q); }
  const float var = wave_sum(q) * (1.0f / DM);
  const float mb = rbf(mean), sd = rbf(sqrtf(rbf(rbf(var) + 1e-5f)));
  const float ri = 1.0f / sd;
  u32x4 o;
#pragma unroll
  for (int k = 0; k < 4; ++k) {
    const float d0 = rbf(e[2 * k] - mb), d1 = rbf(e[2 * k + 1] - mb);
    float q0 = d0 * ri, q1 = d1 * ri;
    q0 = fmaf(fmaf(-q0, sd, d0), ri, q0); q1 = fmaf(fmaf(-q1, sd, d1), ri, q1);
    o[k] = pack2(q0, q1);
  }
  return o;
}

#define STAMP(i)
#define STAMP_DECL
#define STAMP_FLUSH

constexpr int L_QP = 0;
constexpr int L_A = L_QP + 4 * DH;
constexpr int L_CNT = L_A + 1024, L_TAG = L_CNT + 1, L_MID = L_CNT + 2, L_T0 = L_CNT + 3, L_FW = L_CNT + 4;
constexpr int L_RED = L_CNT + 16;
constexpr int L_XN = L_RED + 128;
constexpr int L_GU = L_XN + 4 * 128;
constexpr int L_H = L_GU + 4 * 256;
constexpr int L_S = L_H + 4 * 64;
constexpr int L_TOT = L_S + 4 * NREVMAX * 64;

__global__ void __launch_bounds__(NTHR, 2) attn_kernel(const u16* __restrict__ kc, const u16* __restrict__ vc, const u16* __restrict__ x,
                                                       const u16* __restrict__ wq, unsigned* ws, u16* __restrict__ out,
                                                       const u16* __restrict__ wo, const u16* __restrict__ w2,
                                                       const u16* __restrict__ w1, const u16* __restrict__ w3) {
  __shared__ __attribute__((aligned(16))) unsigned lds[L_TOT];
  STAMP_DECL; STAMP(0);
  const int bid = blockIdx.x;
  const int tid = threadIdx.x, lane = tid & 63, wv = __builtin_amdgcn_readfirstlane(tid >> 6);
  const int j = bid >> 3, h = (bid & 7) * 4 + (j >> 3), b = (j >> 2) & 1, s = j & 3;
  const int r = lane >> 4, c = lane & 15;
  const bool fgrp = !(ABL & 2) && (bid & 1) == 0 && (j >> 2) < 7;
  const int w = wv - 4;
  const int fs = fgrp ? FSH : 0;
  int nst = NSTEP + (s == 0 ? -3 * fs : fs);
  int kvb = ((b * NH + h) * NSPLIT * NSTEP + s * NSTEP + (s == 0 ? 0 : (s - 4) * fs)) * STEPB;
  int so0 = w * 4096 + ((2 * s + b) * 8 + (s == 0 ? 0 : (4 - s) * fs)) * STEPB;
  int st0 = s * NSTEP + (s == 0 ? 0 : (s - 4) * fs);
  kvb = (b * NH + h) * NSPLIT * NSTEP * STEPB;
  so0 = s * NSTEP + (2 * s + b) * 8;
  asm volatile("" : "+s"(st0));
  asm volatile("" : "+s"(nst), "+s"(kvb), "+s"(so0) : "s"(kc), "s"(vc), "s"(x), "s"(wq), "s"(ws), "s"(out));

  const unsigned tb = (unsigned)__builtin_amdgcn_s_memrealtime() | 1u;
  if (wv < 4) {
    const int a = wv;
    __builtin_amdgcn_s_setprio(3);
    const rsrc_t rx = mkrsrc(x, NB * DM * 2);
    const u32x4 xa = bldc(rx, lane * 16, b * (DM * 2));
    u32x4 wr[32];
    const rsrc_t rq = mkrsrc(wq + h * DH, (DM * NH * DH - h * DH) * 2);
    const int vq = r * (8 * NH * DH * 2) + c * 16;
#define LD_WQ(i) { if ((i) >= WQ_INFL) asm volatile("s_waitcnt vmcnt(%0)" ::"n"(WQ_INFL - 1) : "memory"); \
      wr[i] = (ABL & 8) ? xa : bldc(rq, vq, (128 * a + 32 * ((((i) >> 3) + s) & 3) + ((i) & 7)) * (NH * DH * 2)); SB(); }
#pragma unroll
    for (int i = 0; i < WQ_INFL; ++i) LD_WQ(i)
    STAMP(1);
    if (a == 0) {
      const unsigned t = load_tag(ws);
      if (lane == 0) { lds[L_CNT] = 0u; lds[L_TAG] = t; lds[L_MID] = 0u; lds[L_T0] = 0u; }
      if (bid == 0) gst(mkrsrc(ws, WS_END), lane < 8 ? 1024 + lane * 128 : WS_END, 0, __uint_as_float(tb), __uint_as_float(tb), t);
    }
    SB();
    unsigned* xn = lds + L_XN + a * 128;
    {
      const u32x4 n0 = layernorm8(xa);
      if (r == a) *(u32x4*)(xn + 64 * b + 4 * c) = n0;
    }
    SB();
    STAMP(7);
    f32x4 d[8];
#pragma unroll
    for (int e = 0; e < 8; ++e) d[e] = f32x4{0.f, 0.f, 0.f, 0.f};
#pragma unroll
    for (int ks = 0; ks < 4; ++ks) {
#pragma unroll
      for (int i = 8 * ks + WQ_INFL; i < 8 * ks + 8 + WQ_INFL; ++i) if (i < 32) LD_WQ(i)
      const u32x4 af = *(const u32x4*)(xn + 64 * b + 16 * ((ks + s) & 3) + 4 * r);
#pragma unroll
      for (int e = 0; e < 8; ++e) d[e] = mfma(af, bfrag(wr + 8 * ks, e), d[e]);
      SB();
    }
#undef LD_WQ
    if (r == 0) {
      *(f32x4*)(lds + L_QP + a * DH + 8 * c) = f32x4{d[0][0], d[1][0], d[2][0], d[3][0]};
      *(f32x4*)(lds + L_QP + a * DH + 8 * c + 4) = f32x4{d[4][0], d[5][0], d[6][0], d[7][0]};
    }
    LGKM0();
    STAMP(6);
    __builtin_amdgcn_s_barrier();
    const unsigned tag = lds[L_TAG];
    const rsrc_t rw = mkrsrc(ws, WS_END);
    if (a == 1) {
      const u32x4 g = gld(rw, 1024 + (bid & 7) * 128, 0);
      if (g[1] == tag && tb - g[0] < 200u) *(volatile LAS unsigned*)(lds + L_T0) = g[0];
    }
    u32x4 bf[32];
    {
      const rsrc_t ro = mkrsrc(wo + (size_t)h * DH * DM, DH * DM * 2);
#pragma unroll
      for (int ks = 0; ks < 4; ++ks) {
#pragma unroll
        for (int i = 0; i < 8; ++i) {
          __builtin_amdgcn_s_sleep(WO_SLEEP);
          if (!(ABL & 1)) wr[i] = __builtin_amdgcn_raw_buffer_load_b128(ro, r * 8192 + c * 16 + (i & 3) * 1024, ks * 32768 + (i >> 2) * 4096 + a * 256, OAUX);
          SB();
        }
#pragma unroll
        for (int e = 0; e < 8; ++e) { bf[8 * ks + e] = bfrag(wr, e); asm volatile("" : "+v"(bf[8 * ks + e])); }
        SB();
      }
    }
    if (fgrp && s == 0) {
      const int sl = ((bid & 7) >> 1) * 7 + (j >> 2), f0 = sl * FSL;
      const u32x4 xo = bldc(rx, lane * 16, (b ^ 1) * (DM * 2));
      const GAS u32x4* p13 = (const GAS u32x4*)((c < 8 ? w1 : w3) + (size_t)(128 * a + 8 * r) * FF + f0 + 8 * (c & 7));
#pragma unroll
      for (int i = 0; i < 8; ++i) { FSLEEP(); wr[i] = __builtin_nontemporal_load(p13 + i * (FF / 8)); SB(); }
      {
        const u32x4 n1 = layernorm8(xo);
        if (r == a) *(u32x4*)(xn + 64 * (b ^ 1) + 4 * c) = n1;
      }
#pragma unroll
      for (int e = 0; e < 8; ++e) d[e] = f32x4{0.f, 0.f, 0.f, 0.f};
      const rsrc_t r2 = mkrsrc(w2 + (size_t)f0 * DM, FSL * DM * 2);
#pragma unroll
      for (int ks = 0; ks < 4; ++ks) {
        const u32x4 af = *(const u32x4*)(xn + 64 * (c & 1) + 16 * ks + 4 * r);
#pragma unroll
        for (int e = 0; e < 8; ++e) d[e] = mfma(af, bfrag(wr, e), d[e]);
        SB();
#pragma unroll
        for (int jj = 0; jj < 8; ++jj) {
          FSLEEP();
          wr[jj] = ks < 3 ? __builtin_nontemporal_load(p13 + (32 * (ks + 1) + jj) * (FF / 8))
                          : bld(r2, r * 8192 + c * 16 + (jj & 3) * 1024, (jj >> 2) * 4096 + a * 256);
          SB();
        }
      }
      if (r < 2) {
        float* gu = (float*)lds + L_GU + a * 256 + r * 128 + 8 * c;
        *(f32x4*)(gu) = r ? f32x4{d[0][1], d[1][1], d[2][1], d[3][1]} : f32x4{d[0][0], d[1][0], d[2][0], d[3][0]};
        *(f32x4*)(gu + 4) = r ? f32x4{d[4][1], d[5][1], d[6][1], d[7][1]} : f32x4{d[4][0], d[5][0], d[6][0], d[7][0]};
      }
      LGKM0();
      if (lane == 0) __hip_atomic_fetch_add((LAS unsigned*)(lds + L_CNT), 1u, __ATOMIC_RELAXED, __HIP_MEMORY_SCOPE_WORKGROUP);
      while (*(volatile LAS unsigned*)(lds + L_CNT) < 4u) __builtin_amdgcn_s_sleep(1);
      asm volatile("" ::: "memory");
      {
        const float* gu = (const float*)lds + L_GU + (lane >> 5) * 128 + 2 * (lane & 31);
        f32x2 g = {0.f, 0.f}, u = {0.f, 0.f};
#pragma unroll
        for (int aa = 0; aa < 4; ++aa) { g += *(const f32x2*)(gu + aa * 256); u += *(const f32x2*)(gu + aa * 256 + 64); }
        float hv[2];
#pragma unroll
        for (int k = 0; k < 2; ++k) { const float gg = rbf(g[k]); hv[k] = rbf(gg / (1.0f + __expf(-gg))) * rbf(u[k]); }
        lds[L_H + a * 64 + lane] = pack2(hv[0], hv[1]);
      }
#pragma unroll
      for (int e = 0; e < 8; ++e) d[e] = f32x4{0.f, 0.f, 0.f, 0.f};
#pragma unroll
      for (int ks = 0; ks < 2; ++ks) {
        const u32x4 af = *(const u32x4*)(lds + L_H + a * 64 + 32 * (c & 1) + 16 * ks + 4 * r);
#pragma unroll
        for (int e = 0; e < 8; ++e) d[e] = mfma(af, bfrag(wr, e), d[e]);
        SB();
        if (ks == 0) {
#pragma unroll
          for (int jj = 0; jj < 8; ++jj) { FSLEEP(); wr[jj] = bld(r2, r * 8192 + c * 16 + (jj & 3) * 1024, 32768 + (jj >> 2) * 4096 + a * 256); SB(); }
        }
      }
      {
        const int fo = WS_F + (((r & 1) * 32 + 8 * a + (c >> 1)) * NFW + sl) * 128 + (c & 1) * 64 + (r >> 1) * 32;
        float t[4];
#pragma unroll
        for (int e = 0; e < 4; ++e) {
          const float p0 = (r & 1) ? d[e][1] : d[e][0], p1 = (r & 1) ? d[4 + e][1] : d[4 + e][0];
          t[e] = r < 2 ? p0 : p1;
        }
        gst(rw, fo, 0, t[0], t[1], tag); gst(rw, fo + 16, 0, t[2], t[3], tag);
      }
      SB();
    }
    if (a == 0) {
      while (*(volatile LAS unsigned*)(lds + L_MID) < 4u) __builtin_amdgcn_s_sleep(16);
      if (!(fgrp && s == 0)) for (int i = 0; i < ML_SLEEP; ++i) __builtin_amdgcn_s_sleep(127);
      u32x4 ml;
      for (unsigned spin = 0; spin < SPIN_CAP; ++spin) {
        asm volatile("" ::: "memory");
        ml = gld(rw, c * 16, WS_ML + (b * NH + h) * 256);
        if (__builtin_amdgcn_ballot_w64(((ml[1] ^ tag) | (ml[3] ^ tag)) != 0u) == 0ull) break;
        __builtin_amdgcn_s_sleep(64);
      }
      const float mw = __uint_as_float(ml[0]), M = row16_max(mw);
      const float e = __builtin_amdgcn_exp2f(mw - M);
      const float f = e / row16_sum(e * __uint_as_float(ml[2]));
      if ((lane >> 2) == s) lds[L_FW + (lane & 3)] = __float_as_uint(f);
      LGKM0();
    }
    STAMP(4);
#pragma nounroll
    for (int pass = DRY ? 0 : 1; pass < 2; ++pass) {
    if (pass) __builtin_amdgcn_s_barrier();
    asm volatile("" ::: "memory");
    u32x4 af[4];
#pragma unroll
    for (int ks = 0; ks < 4; ++ks) af[ks] = *(const u32x4*)(lds + L_A + (ks * 64 + r * 16 + (c & 7)) * 4);
    const f32x2 fw = *(const f32x2*)(lds + L_FW + 2 * (r & 1));
    const float f0 = fw[0], f1 = fw[1];
#pragma unroll
    for (int e = 0; e < 8; ++e) d[e] = f32x4{0.f, 0.f, 0.f, 0.f};
#pragma unroll
    for (int ks = 0; ks < 4; ++ks)
#pragma unroll
      for (int e = 0; e < 8; ++e) d[e] = mfma(af[ks], bf[8 * ks + e], d[e]);
    float v[4];
#pragma unroll
    for (int e = 0; e < 4; ++e) {
      const float p0 = d[e][0] + d[e][1], p1 = d[e][2] + d[e][3], q0 = d[4 + e][0] + d[4 + e][1], q1 = d[4 + e][2] + d[4 + e][3];
      v[e] = f0 * (r < 2 ? p0 : q0) + f1 * (r < 2 ? p1 : q1);
    }
    gst(rw, (c >> 1) * (128 * 128) + (c & 1) * 64 + r * 16, pass == 0 ? WS_END : WS_Y + ((b * 32 + 8 * a) * 128 + h * NSPLIT + s) * 128,
        t16_sum(v[0], v[2]), t16_sum(v[1], v[3]), tag);
    }
    STAMP(3); STAMP_FLUSH;
    return;
  }

  u32x4 ring[16 * (1 + NXR)];
  const int vo = lane * 16 + (w & 1) * 8192 + (w >> 1) * (PAT == 3 || PAT == 4 ? 65536 : 32768);
  constexpr int lim = NSPLIT * NSTEP * STEPB;
  const int send = st0 + nst;
  const rsrc_t rk = mkrsrc((const char*)kc + (unsigned)kvb, lim), rv = mkrsrc((const char*)vc + (unsigned)kvb, lim);
  int so = so0;
  int tstart = (int)tb;
  int dt = ((T1 + LEAD) * 64) / nst, tg = -(LEAD + (fgrp && s == 0 ? FFN_AHEAD : 0)) * 64;
#define PACE(g) if ((g) % PGR == 0) { while (((int)__builtin_amdgcn_s_memrealtime() - tstart) * 64 < tg) __builtin_amdgcn_s_sleep(PSL); } tg += dt;
#define ADV() ++so; so = so >= send ? st0 : so;
#define SO (((so >> 2) << 16) | ((so & 1) << 12) | ((so & 2) << 13))
#define FILL(rs, g, o) _Pragma("unroll") for (int i = 0; i < 4; ++i) { ring[(g) * 4 + i] = bld(rs, vo + i * 1024, o); SB(); }
#pragma unroll
  for (int g = 4; g < 4 + 4 * NXR; ++g) { if (PB && g - 4 >= PB) { PACE(0) } else tg += dt; FILL(rk, g, SO) if (g == 4) STAMP(5); ADV() }
#pragma unroll
  for (int g = 0; g < 4; ++g) { if (PB) { PACE(0) } else tg += dt; FILL(rk, g, SO) ADV() }
  STAMP(6);
  __builtin_amdgcn_s_barrier();
  unsigned qp[4];
  {
    f32x4 t0 = *(const f32x4*)(lds + L_QP + 8 * c), t1 = *(const f32x4*)(lds + L_QP + 8 * c + 4);
#pragma unroll
    for (int aa = 1; aa < 4; ++aa) {
      t0 += *(const f32x4*)(lds + L_QP + aa * DH + 8 * c);
      t1 += *(const f32x4*)(lds + L_QP + aa * DH + 8 * c + 4);
    }
    qp[0] = pack2(t0[0], t0[1]); qp[1] = pack2(t0[2], t0[3]); qp[2] = pack2(t1[0], t1[1]); qp[3] = pack2(t1[2], t1[3]);
  }
  STAMP(1);
  constexpr float SCALE = 0.088388347648318440f, LOG2E = 1.4426950408889634f;
  float* sl = (float*)lds + L_S + w * (NREVMAX * 64) + lane;
  float m = -INFINITY;
  float xs[16];
#define QK(g, slot)                                                                                  \
    _Pragma("unroll") for (int i = 0; i < 4; ++i) {                                                  \
      float d_ = 0.f;                                                                                \
      _Pragma("unroll") for (int k = 0; k < 4; ++k) d_ = dot2(ring[(slot) * 4 + i][k], qp[k], d_);   \
      xs[(g) * 4 + i] = d_;                                                                          \
    }                                                                                                \
    PIN4(xs + (g) * 4);
#define TR(HALF, CTRL) _Pragma("unroll") for (int k = 0; k < (HALF); ++k) {                          \
      const float t1_ = xs[k] + dpp<CTRL>(xs[k]), t2_ = xs[k + (HALF)] + dpp<CTRL>(xs[k + (HALF)]);  \
      xs[k] = (c & (HALF)) ? t2_ : t1_; }
#define SCORE(rev, nvalid)                                                                           \
  {                                                                                                  \
    TR(8, XOR8) TR(4, XOR7) TR(2, XOR2) TR(1, XOR1)                                                  \
    float t = rbf(rbf(xs[0]) * SCALE) * LOG2E;                     \
    t = (c >> 2) < (nvalid) ? t : -INFINITY;                                                         \
    m = fmaxf(m, t);                                                                                 \
    sl[(rev) * 64] = t;                                                                              \
    SB();                                                                                            \
  }
  const int nrev = (nst + 3) >> 2;
  {
    int left = nst - 4 * (1 + NXR);
    rsrc_t rn = rk;
#pragma nounroll
    for (int rev = NXR; rev < nrev; ++rev) {
      if (rev == nrev - 1) { rn = rv; so = so0; left = 4; tg = T1 * 64; dt = ((T2 - T1 - (fgrp && s == 0 ? FFN_AHEAD2 : (bid & 1) == 0 && j < 16 ? CONS_AHEAD : (bid & 1) == 0 ? EVEN_AHEAD : 0)) * 64) / nst; }
#define STEP(g) { const int o_ = left > (g) ? SO : lim; QK(g, g) if (!P1FIX || left > (g)) { PACE(g) } FILL(rn, g, o_) ADV() }
      STEP(0) STEP(1) STEP(2) STEP(3)
#undef STEP
      SCORE(rev, nst - 4 * rev)
      { const int t0_ = __builtin_amdgcn_readfirstlane(*(volatile LAS unsigned*)(lds + L_T0)); tstart = t0_ ? t0_ : tstart; }
      left -= 4;
#define XREV(e) if constexpr ((e) < NXR) { if (rev == NXR + (e)) {                                   \
        _Pragma("unroll") for (int g = 0; g < 4; ++g) { PIN4(ring + (4 * (1 + (e)) + g) * 4); QK(g, 4 * (1 + (e)) + g) } \
        SCORE(e, 4) } }
      XREV(0) XREV(1)
#undef XREV
    }
  }
  {
    m = xrow_max(row16_max(m));
    float l = 0.f;
#pragma unroll
    for (int rev = 0; rev < NREVMAX; ++rev) {
      const float t = rev < NREVMAX - 3 || rev < nrev ? sl[rev * 64] : -INFINITY;
      const float p = __builtin_amdgcn_exp2f(t - m);
      l += p;
      sl[rev * 64] = p;
    }
    l = wave_sum(l);
    gst(mkrsrc(ws, WS_END), lane == 0 ? 0 : WS_END, WS_ML + (b * NH + h) * 256 + (4 * s + w) * 16, m, l, lds[L_TAG]);
    if (lane == 0) __hip_atomic_fetch_add((LAS unsigned*)(lds + L_MID), 1u, __ATOMIC_RELAXED, __HIP_MEMORY_SCOPE_WORKGROUP);
    SB();
  }
  STAMP(3);
  f32x2 acc[4];
#pragma unroll
  for (int k = 0; k < 4; ++k) acc[k] = f32x2{0.f, 0.f};
#define PV1(g, i) { const float pb = dpp<BCAST + 4 * (g) + (i)>(p); const u32x4 vv = ring[(g) * 4 + (i)];  \
      _Pragma("unroll") for (int k = 0; k < 4; ++k)                                                  \
        acc[k] = __builtin_elementwise_fma(f32x2{pb, pb}, f32x2{blo(vv[k]), bhi(vv[k])}, acc[k]); }
#define PV(g) PV1(g, 0) PV1(g, 1) PV1(g, 2) PV1(g, 3) PIN4(acc);
  int left = nst - 4;
  float pn = sl[0];
#pragma nounroll
  for (int rev = 1; rev < nrev; ++rev) {
    const float p = pn;
    pn = sl[rev * 64];
#define STEP(g) { const int o_ = left > (g) ? SO : lim; PV(g) if (left > (g) + SPRINT) { PACE(g) } FILL(rv, g, o_) ADV() }
    STEP(0) STEP(1) STEP(2) STEP(3)
#undef STEP
    left -= 4;
  }
  {
    const float p = pn;
    PV(0)
    if (left > -3) { PV(1) }
    if (left > -2) { PV(2) }
    if (left > -1) { PV(3) }
  }
  STAMP(2);
  {
    const float a0 = t32_sum(acc[0][0], acc[2][0]), a1 = t32_sum(acc[0][1], acc[2][1]);
    const float a2 = t32_sum(acc[1][0], acc[3][0]), a3 = t32_sum(acc[1][1], acc[3][1]);
    const float t0 = t16_sum(a0, a2), t1 = t16_sum(a1, a3);
    const unsigned hi = pack2(t0, t1), lo = pack2(t0 - blo(hi), t1 - bhi(hi));
    unsigned* pa = lds + L_A + ((c >> 2) * 64 + (c & 3) * 16 + 2 * w) * 4 + r;
    pa[0] = hi; pa[4] = lo;
  }
  LGKM0();
  STAMP(4);
  __builtin_amdgcn_s_barrier();
  if (!(ABL & 6) && (bid & 1) == CONS_PAR && j < 16) {
    const int ci = ((bid & 7) >> 1) * 16 + j, cb = ci >> 5, cg = ci & 31;
    const int t = tid - 256, pc = t & 7, hh = t >> 3;
    const unsigned tag = lds[L_TAG];
    const rsrc_t rw = mkrsrc(ws, WS_END);
    const int sy = WS_Y + (cb * 32 + cg) * (128 * 128);
    int vy = hh * 512 + pc * 16;
    int vf = hh < NFW ? WS_F + ((cb * 32 + cg) * NFW + hh) * 128 + pc * 16 : sy + vy;
    int vx = ci == 0 ? WS_ML + NH * 256 + (t & 127) * 64 : vf;
    const unsigned xv = *(const unsigned*)(x + cb * DM + cg * 16 + 2 * pc);
    float k0 = 0.f, k1 = 0.f, g0 = 0.f, g1 = 0.f, a0 = 0.f;
    bool done = false;
    u32x4 ya[5], yb[5];
    unsigned yat, ybt;
#define SWEEP(Y) { _Pragma("unroll") for (int i = 0; i < 4; ++i) Y[i] = gld(rw, vy + i * 128, sy);    \
      Y[4] = gld(rw, vf, 0); Y##t = __builtin_amdgcn_raw_buffer_load_b32(rw, vx, 4, 16); asm volatile("" ::: "memory"); SB(); }
#define CHECK(Y) {                                                                                   \
      unsigned bad = Y##t ^ tag;                                                                    \
      _Pragma("unroll") for (int i = 0; i < 5; ++i) bad |= (Y[i][1] ^ tag) | (Y[i][3] ^ tag);        \
      const bool take = !done && bad == 0u;                                                          \
      k0 = take ? (__uint_as_float(Y[0][0]) + __uint_as_float(Y[1][0])) + (__uint_as_float(Y[2][0]) + __uint_as_float(Y[3][0])) : k0; \
      k1 = take ? (__uint_as_float(Y[0][2]) + __uint_as_float(Y[1][2])) + (__uint_as_float(Y[2][2]) + __uint_as_float(Y[3][2])) : k1; \
      g0 = take && hh < NFW ? __uint_as_float(Y[4][0]) : g0; g1 = take && hh < NFW ? __uint_as_float(Y[4][2]) : g1; \
      done = done || take;                                                                           \
      vy = done ? WS_END : vy; vf = done ? WS_END : vf; vx = done ? WS_END : vx;                     \
      a0 = t32_sum(k0 + dpp<XOR8>(k0), g0 + dpp<XOR8>(g0));        \
      a0 = t16_sum(a0, t32_sum(k1 + dpp<XOR8>(k1), g1 + dpp<XOR8>(g1)));     \
      const unsigned long long miss = __builtin_amdgcn_ballot_w64(!done);                            \
      if (miss == 0ull) break;                                                                       \
      if (__builtin_popcountll(miss) > POLL_MANY) __builtin_amdgcn_s_sleep(POLL_SLEEP);              \
      asm volatile("" ::: "memory"); }
    SWEEP(ya)
    __builtin_amdgcn_s_sleep(12);
    for (unsigned spin = 0; spin < SPIN_CAP; ++spin) { SWEEP(yb) CHECK(ya) SWEEP(ya) CHECK(yb) }
#undef SWEEP
#undef CHECK
    if (c < 8) lds[L_RED + w * 32 + (r >> 1) * 16 + 2 * c + (r & 1)] = __float_as_uint(a0);
    LGKM0();
    unsigned tk = 0;
    if (lane == 0) tk = __hip_atomic_fetch_add((LAS unsigned*)(lds + L_CNT), 16u, __ATOMIC_RELAXED, __HIP_MEMORY_SCOPE_WORKGROUP);
    if (__builtin_amdgcn_readfirstlane(tk) >= 48u) {
      asm volatile("" ::: "memory");
      if (lane < 8) {
        const float* red = (const float*)lds + L_RED + 2 * lane;
        const f32x2 A = (*(const f32x2*)(red) + *(const f32x2*)(red + 32)) + (*(const f32x2*)(red + 64) + *(const f32x2*)(red + 96));
        const f32x2 F = (*(const f32x2*)(red + 16) + *(const f32x2*)(red + 48)) + (*(const f32x2*)(red + 80) + *(const f32x2*)(red + 112));
        *(unsigned*)(out + cb * DM + cg * 16 + 2 * lane) = pack2(rbf(blo(xv) + rbf(A[0])) + rbf(F[0]), rbf(bhi(xv) + rbf(A[1])) + rbf(F[1]));
      }
      if (ci == 0 && lane == 0) __builtin_amdgcn_raw_buffer_store_b32(tag, rw, WS_E, 0, 16);
    }
    STAMP(7);
    asm volatile("" ::"v"(ya[0]), "v"(ya[1]), "v"(ya[2]), "v"(ya[3]), "v"(ya[4]), "v"(yat), "v"(yb[0]), "v"(yb[1]), "v"(yb[2]), "v"(yb[3]), "v"(yb[4]), "v"(ybt) : "memory");
    STAMP_FLUSH;
    return;
  }
  STAMP(7);
  STAMP_FLUSH;
}

}

extern "C" void kernel_launch(void* const* d_in, const int* in_sizes, int n_in, void* d_out, int out_size, void* d_ws,
                              size_t ws_size, hipStream_t stream) {
  attn_kernel<<<NWG, NTHR, 0, stream>>>((const u16*)d_in[1], (const u16*)d_in[2], (const u16*)d_in[0], (const u16*)d_in[3], (unsigned*)d_ws,
                                        (u16*)d_out, (const u16*)d_in[4], (const u16*)d_in[8], (const u16*)d_in[6], (const u16*)d_in[7]);
}
```

```cpp
#include <hip/hip_runtime.h>
#include <hip/hip_bf16.h>
#include <cstdint>

namespace {

constexpr int NB = 2, DM = 512, NH = 32, SK = 16384, DH = 128, FF = 1792;
constexpr int NSPLIT = 4;
constexpr int NTHR = 512;
constexpr int FSH = 2;
constexpr int NXR = 2;
constexpr int T1 = 3785, T2 = 7480, AH_FFN1 = 300, AH_FFN2 = 100, AH_CONS = 80;
constexpr int WQ_INFL = 8;
constexpr int NWG = NB * NH * NSPLIT;
constexpr int STEPK = 64;
constexpr int NSTEP = SK / NSPLIT / STEPK;
constexpr int NREVMAX = (NSTEP + FSH + 3) / 4;
constexpr int FSL = 64, NFW = FF / FSL;

constexpr int WS_E = 0;
constexpr int WS_T = 1024;
constexpr int WS_ML = 4096;
constexpr int WS_F = WS_ML + NB * NH * 256;
constexpr int WS_Y = WS_F + NB * 32 * NFW * 128;
constexpr int WS_END = WS_Y + NB * 32 * 128 * 128;
constexpr unsigned SPIN_CAP = 1u << 20;

typedef unsigned u32x4 __attribute__((ext_vector_type(4)));
typedef float f32x4 __attribute__((ext_vector_type(4)));
typedef float f32x2 __attribute__((ext_vector_type(2)));
typedef __bf16 bf16x2 __attribute__((ext_vector_type(2)));
typedef __bf16 bf16x8 __attribute__((ext_vector_type(8)));
typedef unsigned short u16;
#define GAS __attribute__((address_space(1)))
#define LAS __attribute__((address_space(3)))

__device__ __forceinline__ float dot2(unsigned a, unsigned b, float acc) {
  return __builtin_amdgcn_fdot2_f32_bf16(__builtin_bit_cast(bf16x2, a), __builtin_bit_cast(bf16x2, b), acc, false);
}
__device__ __forceinline__ float blo(unsigned u) { return __builtin_bit_cast(float, u << 16); }
__device__ __forceinline__ float bhi(unsigned u) { return __builtin_bit_cast(float, u & 0xffff0000u); }
__device__ __forceinline__ float rbf(float f) { return (float)(__bf16)f; }
__device__ __forceinline__ unsigned pack2(float lo, float hi) {
  bf16x2 v; v.x = (__bf16)lo; v.y = (__bf16)hi; return __builtin_bit_cast(unsigned, v);
}
template <int CTRL> __device__ __forceinline__ float dpp(float x) {
  return __builtin_bit_cast(float, __builtin_amdgcn_mov_dpp(__builtin_bit_cast(int, x), CTRL, 0xf, 0xf, true));
}
constexpr int XOR1 = 0xB1, XOR2 = 0x4E, XOR7 = 0x141, XOR8 = 0x128, BCAST = 0x150;
__device__ __forceinline__ float row16_sum(float x) {
  x += dpp<XOR1>(x); x += dpp<XOR2>(x); x += dpp<XOR7>(x); x += dpp<XOR8>(x); return x;
}
__device__ __forceinline__ float row16_max(float x) {
  x = fmaxf(x, dpp<XOR1>(x)); x = fmaxf(x, dpp<XOR2>(x)); x = fmaxf(x, dpp<XOR7>(x)); return fmaxf(x, dpp<XOR8>(x));
}
__device__ __forceinline__ float t16_sum(float a, float b) {
  auto s = __builtin_amdgcn_permlane16_swap(__float_as_uint(a), __float_as_uint(b), false, false);
  return __uint_as_float(s[0]) + __uint_as_float(s[1]);
}
__device__ __forceinline__ float t32_sum(float a, float b) {
  auto s = __builtin_amdgcn_permlane32_swap(__float_as_uint(a), __float_as_uint(b), false, false);
  return __uint_as_float(s[0]) + __uint_as_float(s[1]);
}
__device__ __forceinline__ float xrow_sum(float x) { x = t16_sum(x, x); return t32_sum(x, x); }
__device__ __forceinline__ float xrow_max(float x) {
  auto s = __builtin_amdgcn_permlane16_swap(__float_as_uint(x), __float_as_uint(x), false, false);
  x = fmaxf(__uint_as_float(s[0]), __uint_as_float(s[1]));
  auto t = __builtin_amdgcn_permlane32_swap(__float_as_uint(x), __float_as_uint(x), false, false);
  return fmaxf(__uint_as_float(t[0]), __uint_as_float(t[1]));
}
__device__ __forceinline__ float wave_sum(float x) { return xrow_sum(row16_sum(x)); }
typedef __amdgpu_buffer_rsrc_t rsrc_t;
__device__ __forceinline__ rsrc_t mkrsrc(const void* p, int bytes) { return __builtin_amdgcn_make_buffer_rsrc((void*)p, 0, bytes, 0x00020000); }
__device__ __forceinline__ u32x4 bld(rsrc_t r, int voff, int soff) { return __builtin_amdgcn_raw_buffer_load_b128(r, voff, soff, 2); }
__device__ __forceinline__ u32x4 bldc(rsrc_t r, int voff, int soff) { return __builtin_amdgcn_raw_buffer_load_b128(r, voff, soff, 0); }
__device__ __forceinline__ u32x4 gld(rsrc_t r, int voff, int soff) { return __builtin_amdgcn_raw_buffer_load_b128(r, voff, soff, 16); }
__device__ __forceinline__ void gst(rsrc_t r, int voff, int soff, float v0, float v1, unsigned tag) {
  __builtin_amdgcn_raw_buffer_store_b128(u32x4{__float_as_uint(v0), tag, __float_as_uint(v1), tag}, r, voff, soff, 16);
}
#define SB() __builtin_amdgcn_sched_barrier(0)
__device__ __forceinline__ unsigned load_tag(const unsigned* ws) {
  unsigned e;
  asm volatile("s_load_dword %0, %1, 0x0\n\ts_waitcnt lgkmcnt(0)" : "=s"(e) : "s"(ws) : "memory");
  return e + 1u;
}
#define PIN4(a) asm volatile("" : "+v"((a)[0]), "+v"((a)[1]), "+v"((a)[2]), "+v"((a)[3])::"memory"); SB()
#define LGKM0() asm volatile("s_waitcnt lgkmcnt(0)" ::: "memory")
#define CFENCE() asm volatile("" ::: "memory")
#define BARRIER() do { LGKM0(); __builtin_amdgcn_s_barrier(); CFENCE(); } while (0)
__device__ __forceinline__ f32x4 mfma(u32x4 a, u32x4 b, f32x4 c) {
  return __builtin_amdgcn_mfma_f32_16x16x32_bf16(__builtin_bit_cast(bf16x8, a), __builtin_bit_cast(bf16x8, b), c, 0, 0, 0);
}
__device__ __forceinline__ u32x4 bfrag(const u32x4* w, int e) {
  u32x4 o;
#pragma unroll
  for (int jj = 0; jj < 4; ++jj) o[jj] = __builtin_amdgcn_perm(w[2 * jj + 1][e >> 1], w[2 * jj][e >> 1], (e & 1) ? 0x07060302u : 0x05040100u);
  return o;
}
__device__ __forceinline__ u32x4 layernorm8(u32x4 xa) {
  float e[8];
#pragma unroll
  for (int k = 0; k < 4; ++k) { e[2 * k] = blo(xa[k]); e[2 * k + 1] = bhi(xa[k]); }
  const float mean = wave_sum(((e[0] + e[1]) + (e[2] + e[3])) + ((e[4] + e[5]) + (e[6] + e[7]))) * (1.0f / DM);
  float q = 0.f;
#pragma unroll
  for (int k = 0; k < 8; ++k) { const float d = e[k] - mean; q = fmaf(d, d, q); }
  const float var = wave_sum(q) * (1.0f / DM);
  const float mb = rbf(mean), sd = rbf(sqrtf(rbf(rbf(var) + 1e-5f)));
  const float ri = 1.0f / sd;
  u32x4 o;
#pragma unroll
  for (int k = 0; k < 4; ++k) {
    const float d0 = rbf(e[2 * k] - mb), d1 = rbf(e[2 * k + 1] - mb);
    float q0 = d0 * ri, q1 = d1 * ri;
    q0 = fmaf(fmaf(-q0, sd, d0), ri, q0); q1 = fmaf(fmaf(-q1, sd, d1), ri, q1);
    o[k] = pack2(q0, q1);
  }
  return o;
}

constexpr int L_QP = 0;
constexpr int L_A = L_QP + 4 * DH;
constexpr int L_CNT = L_A + 1024, L_TAG = L_CNT + 1, L_MID = L_CNT + 2, L_T0 = L_CNT + 3, L_FW = L_CNT + 4;
constexpr int L_RED = L_CNT + 16;
constexpr int L_XN = L_RED + 128;
constexpr int L_GU = L_XN + 4 * 128;
constexpr int L_H = L_GU + 4 * 256;
constexpr int L_S = L_H + 4 * 64;
constexpr int L_TOT = L_S + 4 * NREVMAX * 64;

__global__ void __launch_bounds__(NTHR, 2) attn_kernel(const u16* __restrict__ kc, const u16* __restrict__ vc, const u16* __restrict__ x,
                                                       const u16* __restrict__ wq, unsigned* ws, u16* __restrict__ out,
                                                       const u16* __restrict__ wo, const u16* __restrict__ w2,
                                                       const u16* __restrict__ w1, const u16* __restrict__ w3) {
  __shared__ __attribute__((aligned(16))) unsigned lds[L_TOT];
  const unsigned tb = (unsigned)__builtin_amdgcn_s_memrealtime() | 1u;
  SB();
  const int bid = blockIdx.x;
  const int tid = threadIdx.x, lane = tid & 63, wv = __builtin_amdgcn_readfirstlane(tid >> 6);
  const int j = bid >> 3, h = (bid & 7) * 4 + (j >> 3), b = (j >> 2) & 1, s = j & 3;
  const int r = lane >> 4, c = lane & 15;
  const bool fgrp = (bid & 1) == 0 && (j >> 2) < 7;
  const int w = wv - 4;
  const int fs = fgrp ? FSH : 0;
  int nst = NSTEP + (s == 0 ? -3 * fs : fs), st0 = s * NSTEP + (s == 0 ? 0 : (s - 4) * fs);
  int so0 = s * NSTEP + (((2 * s + b) * 8 + 12 * (j >> 3)) & 63);
  so0 = so0 >= st0 + nst ? so0 - nst : so0;
  int kvb = (b * NH + h) * (SK * DH * 2);
  asm volatile("" : "+s"(nst), "+s"(st0), "+s"(kvb), "+s"(so0) : "s"(kc), "s"(vc), "s"(x), "s"(wq), "s"(ws), "s"(out));

  if (wv < 4) {
    const int a = wv;
    __builtin_amdgcn_s_setprio(3);
    const rsrc_t rx = mkrsrc(x, NB * DM * 2);
    const u32x4 xa = bldc(rx, lane * 16, b * (DM * 2));
    u32x4 wr[32];
    const rsrc_t rq = mkrsrc(wq + h * DH, (DM * NH * DH - h * DH) * 2);
    const int vq = r * (8 * NH * DH * 2) + c * 16;
#define LD_WQ(i) { if ((i) >= WQ_INFL) asm volatile("s_waitcnt vmcnt(%0)" ::"n"(WQ_INFL - 1) : "memory"); \
      wr[i] = bldc(rq, vq, (128 * a + 32 * ((((i) >> 3) + s) & 3) + ((i) & 7)) * (NH * DH * 2)); SB(); }
#pragma unroll
    for (int i = 0; i < WQ_INFL; ++i) LD_WQ(i)
    if (a == 0) {
      const unsigned t = load_tag(ws);
      if (lane == 0) { lds[L_CNT] = 0u; lds[L_TAG] = t; lds[L_MID] = 0u; lds[L_T0] = 0u; }
      if (bid == 0) gst(mkrsrc(ws, WS_END), lane < 8 ? WS_T + lane * 128 : WS_END, 0, __uint_as_float(tb), __uint_as_float(tb), t);
    }
    SB();
    unsigned* xn = lds + L_XN + a * 128;
    {
      const u32x4 n0 = layernorm8(xa);
      if (r == a) *(u32x4*)(xn + 64 * b + 4 * c) = n0;
    }
    CFENCE(); SB();
    f32x4 d[8];
#pragma unroll
    for (int e = 0; e < 8; ++e) d[e] = f32x4{0.f, 0.f, 0.f, 0.f};
#pragma unroll
    for (int ks = 0; ks < 4; ++ks) {
#pragma unroll
      for (int i = 8 * ks + WQ_INFL; i < 8 * ks + 8 + WQ_INFL; ++i) if (i < 32) LD_WQ(i)
      const u32x4 af = *(const u32x4*)(xn + 64 * b + 16 * ((ks + s) & 3) + 4 * r);
#pragma unroll
      for (int e = 0; e < 8; ++e) d[e] = mfma(af, bfrag(wr + 8 * ks, e), d[e]);
      SB();
    }
#undef LD_WQ
    if (r == 0) {
      *(f32x4*)(lds + L_QP + a * DH + 8 * c) = f32x4{d[0][0], d[1][0], d[2][0], d[3][0]};
      *(f32x4*)(lds + L_QP + a * DH + 8 * c + 4) = f32x4{d[4][0], d[5][0], d[6][0], d[7][0]};
    }
    BARRIER();
    const unsigned tag = lds[L_TAG];
    const rsrc_t rw = mkrsrc(ws, WS_END);
    if (a == 1) {
      const u32x4 g = gld(rw, WS_T + (bid & 7) * 128, 0);
      if (g[1] == tag && tb - g[0] < 200u) *(volatile LAS unsigned*)(lds + L_T0) = g[0];
    }
    u32x4 bf[32];
    {
      const rsrc_t ro = mkrsrc(wo + (size_t)h * DH * DM, DH * DM * 2);
#pragma unroll
      for (int ks = 0; ks < 4; ++ks) {
#pragma unroll
        for (int i = 0; i < 8; ++i) {
          __builtin_amdgcn_s_sleep(8);
          wr[i] = bldc(ro, r * 8192 + c * 16 + (i & 3) * 1024, ks * 32768 + (i >> 2) * 4096 + a * 256);
          SB();
        }
#pragma unroll
        for (int e = 0; e < 8; ++e) { bf[8 * ks + e] = bfrag(wr, e); asm volatile("" : "+v"(bf[8 * ks + e])); }
        SB();
      }
    }
    if (fgrp && s == 0) {
      const int sl = ((bid & 7) >> 1) * 7 + (j >> 2), f0 = sl * FSL;
      const u32x4 xo = bldc(rx, lane * 16, (b ^ 1) * (DM * 2));
      const GAS u32x4* p13 = (const GAS u32x4*)((c < 8 ? w1 : w3) + (size_t)(128 * a + 8 * r) * FF + f0 + 8 * (c & 7));
#pragma unroll
      for (int i = 0; i < 8; ++i) wr[i] = __builtin_nontemporal_load(p13 + i * (FF / 8));
      SB();
      {
        const u32x4 n1 = layernorm8(xo);
        if (r == a) *(u32x4*)(xn + 64 * (b ^ 1) + 4 * c) = n1;
      }
      CFENCE();
#pragma unroll
      for (int e = 0; e < 8; ++e) d[e] = f32x4{0.f, 0.f, 0.f, 0.f};
      const rsrc_t r2 = mkrsrc(w2 + (size_t)f0 * DM, FSL * DM * 2);
#pragma unroll
      for (int ks = 0; ks < 4; ++ks) {
        const u32x4 af = *(const u32x4*)(xn + 64 * (c & 1) + 16 * ks + 4 * r);
#pragma unroll
        for (int e = 0; e < 8; ++e) d[e] = mfma(af, bfrag(wr, e), d[e]);
        SB();
#pragma unroll
        for (int jj = 0; jj < 8; ++jj)
          wr[jj] = ks < 3 ? __builtin_nontemporal_load(p13 + (32 * (ks + 1) + jj) * (FF / 8))
                          : bld(r2, r * 8192 + c * 16 + (jj & 3) * 1024, (jj >> 2) * 4096 + a * 256);
        SB();
      }
      if (r < 2) {
        float* gu = (float*)lds + L_GU + a * 256 + r * 128 + 8 * c;
        *(f32x4*)(gu) = r ? f32x4{d[0][1], d[1][1], d[2][1], d[3][1]} : f32x4{d[0][0], d[1][0], d[2][0], d[3][0]};
        *(f32x4*)(gu + 4) = r ? f32x4{d[4][1], d[5][1], d[6][1], d[7][1]} : f32x4{d[4][0], d[5][0], d[6][0], d[7][0]};
      }
      LGKM0();
      if (lane == 0) __hip_atomic_fetch_add((LAS unsigned*)(lds + L_CNT), 1u, __ATOMIC_RELAXED, __HIP_MEMORY_SCOPE_WORKGROUP);
      while (*(volatile LAS unsigned*)(lds + L_CNT) < 4u) __builtin_amdgcn_s_sleep(1);
      CFENCE();
      {
        const float* gu = (const float*)lds + L_GU + (lane >> 5) * 128 + 2 * (lane & 31);
        f32x2 g = {0.f, 0.f}, u = {0.f, 0.f};
#pragma unroll
        for (int aa = 0; aa < 4; ++aa) { g += *(const f32x2*)(gu + aa * 256); u += *(const f32x2*)(gu + aa * 256 + 64); }
        float hv[2];
#pragma unroll
        for (int k = 0; k < 2; ++k) { const float gg = rbf(g[k]); hv[k] = rbf(gg / (1.0f + __expf(-gg))) * rbf(u[k]); }
        lds[L_H + a * 64 + lane] = pack2(hv[0], hv[1]);
      }
      CFENCE();
#pragma unroll
      for (int e = 0; e < 8; ++e) d[e] = f32x4{0.f, 0.f, 0.f, 0.f};
#pragma unroll
      for (int ks = 0; ks < 2; ++ks) {
        const u32x4 af = *(const u32x4*)(lds + L_H + a * 64 + 32 * (c & 1) + 16 * ks + 4 * r);
#pragma unroll
        for (int e = 0; e < 8; ++e) d[e] = mfma(af, bfrag(wr, e), d[e]);
        SB();
        if (ks == 0) {
#pragma unroll
          for (int jj = 0; jj < 8; ++jj) wr[jj] = bld(r2, r * 8192 + c * 16 + (jj & 3) * 1024, 32768 + (jj >> 2) * 4096 + a * 256);
          SB();
        }
      }
      {
        const int fo = WS_F + (((r & 1) * 32 + 8 * a + (c >> 1)) * NFW + sl) * 128 + (c & 1) * 64 + (r >> 1) * 32;
        float t[4];
#pragma unroll
        for (int e = 0; e < 4; ++e) {
          const float p0 = (r & 1) ? d[e][1] : d[e][0], p1 = (r & 1) ? d[4 + e][1] : d[4 + e][0];
          t[e] = r < 2 ? p0 : p1;
        }
        gst(rw, fo, 0, t[0], t[1], tag); gst(rw, fo + 16, 0, t[2], t[3], tag);
      }
      SB();
    }
    if (a == 0) {
      while (*(volatile LAS unsigned*)(lds + L_MID) < 4u) __builtin_amdgcn_s_sleep(16);
      if (!(fgrp && s == 0)) for (int i = 0; i < 3; ++i) __builtin_amdgcn_s_sleep(127);
      u32x4 ml;
      for (unsigned spin = 0; spin < SPIN_CAP; ++spin) {
        CFENCE();
        ml = gld(rw, c * 16, WS_ML + (b * NH + h) * 256);
        if (__builtin_amdgcn_ballot_w64(((ml[1] ^ tag) | (ml[3] ^ tag)) != 0u) == 0ull) break;
        __builtin_amdgcn_s_sleep(64);
      }
      const float mw = __uint_as_float(ml[0]), M = row16_max(mw);
      const float e = __builtin_amdgcn_exp2f(mw - M);
      const float f = e / row16_sum(e * __uint_as_float(ml[2]));
      if ((lane >> 2) == s) lds[L_FW + (lane & 3)] = __float_as_uint(f);
    }
    BARRIER();
    u32x4 af[4];
#pragma unroll
    for (int ks = 0; ks < 4; ++ks) af[ks] = *(const u32x4*)(lds + L_A + (ks * 64 + r * 16 + (c & 7)) * 4);
    const f32x2 fw = *(const f32x2*)(lds + L_FW + 2 * (r & 1));
    const float f0 = fw[0], f1 = fw[1];
#pragma unroll
    for (int e = 0; e < 8; ++e) d[e] = f32x4{0.f, 0.f, 0.f, 0.f};
#pragma unroll
    for (int ks = 0; ks < 4; ++ks)
#pragma unroll
      for (int e = 0; e < 8; ++e) d[e] = mfma(af[ks], bf[8 * ks + e], d[e]);
    float v[4];
#pragma unroll
    for (int e = 0; e < 4; ++e) {
      const float p0 = d[e][0] + d[e][1], p1 = d[e][2] + d[e][3], q0 = d[4 + e][0] + d[4 + e][1], q1 = d[4 + e][2] + d[4 + e][3];
      v[e] = f0 * (r < 2 ? p0 : q0) + f1 * (r < 2 ? p1 : q1);
    }
    gst(rw, (c >> 1) * (128 * 128) + (c & 1) * 64 + r * 16, WS_Y + ((b * 32 + 8 * a) * 128 + h * NSPLIT + s) * 128,
        t16_sum(v[0], v[2]), t16_sum(v[1], v[3]), tag);
    return;
  }

  u32x4 ring[16 * (1 + NXR)];
  const int vo = lane * 16 + (w & 1) * 8192 + (w >> 1) * 32768, send = st0 + nst;
  constexpr int lim = SK * DH * 2;
  const rsrc_t rk = mkrsrc((const char*)kc + (unsigned)kvb, lim), rv = mkrsrc((const char*)vc + (unsigned)kvb, lim);
  int so = so0;
  const bool ffn = fgrp && s == 0, cons = (bid & 1) == 0 && j < 16;
  unsigned tstart = tb;
  constexpr int NF = NSTEP - 3 * FSH, NS = NSTEP + FSH;
  int dt = ffn ? T1 * 64 / NF : fgrp ? T1 * 64 / NS : T1 * 64 / NSTEP;
  int tg = dt * 4 * (1 + NXR) - (ffn ? AH_FFN1 * 64 : 0);
  static_assert(T1 / NF * 4 * (1 + NXR) > AH_FFN1, "");
#define PACE() { while ((unsigned)__builtin_amdgcn_s_memrealtime() - tstart < (unsigned)(tg >> 6)) __builtin_amdgcn_s_sleep(1); tg += dt; }
#define ADV() ++so; so = so >= send ? st0 : so;
#define SO (((so >> 2) << 16) | ((so & 1) << 12) | ((so & 2) << 13))
#define FILL(rs, g, o) _Pragma("unroll") for (int i = 0; i < 4; ++i) { ring[(g) * 4 + i] = bld(rs, vo + i * 1024, o); SB(); }
#pragma unroll
  for (int g = 4; g < 4 + 4 * NXR; ++g) { FILL(rk, g, SO) ADV() }
#pragma unroll
  for (int g = 0; g < 4; ++g) { FILL(rk, g, SO) ADV() }
  BARRIER();
  unsigned qp[4];
  {
    f32x4 t0 = *(const f32x4*)(lds + L_QP + 8 * c), t1 = *(const f32x4*)(lds + L_QP + 8 * c + 4);
#pragma unroll
    for (int aa = 1; aa < 4; ++aa) {
      t0 += *(const f32x4*)(lds + L_QP + aa * DH + 8 * c);
      t1 += *(const f32x4*)(lds + L_QP + aa * DH + 8 * c + 4);
    }
    qp[0] = pack2(t0[0], t0[1]); qp[1] = pack2(t0[2], t0[3]); qp[2] = pack2(t1[0], t1[1]); qp[3] = pack2(t1[2], t1[3]);
  }
  constexpr float SCALE = 0.088388347648318440f, LOG2E = 1.4426950408889634f;
  float* sl = (float*)lds + L_S + w * (NREVMAX * 64) + lane;
  float m = -INFINITY;
  float xs[16];
#define QK(g, slot)                                                                                  \
    _Pragma("unroll") for (int i = 0; i < 4; ++i) {                                                  \
      float d_ = 0.f;                                                                                \
      _Pragma("unroll") for (int k = 0; k < 4; ++k) d_ = dot2(ring[(slot) * 4 + i][k], qp[k], d_);   \
      xs[(g) * 4 + i] = d_;                                                                          \
    }                                                                                                \
    PIN4(xs + (g) * 4);
#define TR(HALF, CTRL) _Pragma("unroll") for (int k = 0; k < (HALF); ++k) {                          \
      const float t1_ = xs[k] + dpp<CTRL>(xs[k]), t2_ = xs[k + (HALF)] + dpp<CTRL>(xs[k + (HALF)]);  \
      xs[k] = (c & (HALF)) ? t2_ : t1_; }
#define SCORE(rev, nvalid)                                                                           \
  {                                                                                                  \
    TR(8, XOR8) TR(4, XOR7) TR(2, XOR2) TR(1, XOR1)                                                  \
    float t = rbf(rbf(xs[0]) * SCALE) * LOG2E;                     \
    t = (c >> 2) < (nvalid) ? t : -INFINITY;                                                         \
    m = fmaxf(m, t);                                                                                 \
    sl[(rev) * 64] = t;                                                                              \
    SB();                                                                                            \
  }
  const int nrev = (nst + 3) >> 2;
  {
    int left = nst - 4 * (1 + NXR);
    rsrc_t rn = rk;
#pragma nounroll
    for (int rev = NXR; rev < nrev; ++rev) {
      if (rev == nrev - 1) {
        rn = rv; so = so0; left = 4;
        tg = T1 * 64;
        dt = ffn ? (T2 - T1 - AH_FFN2) * 64 / NF : cons ? (T2 - T1 - AH_CONS) * 64 / NS : fgrp ? (T2 - T1) * 64 / NS : (T2 - T1) * 64 / NSTEP;
      }
#define STEP(g) { const int o_ = left > (g) ? SO : lim; QK(g, g) if (left > (g)) PACE() FILL(rn, g, o_) ADV() }
      STEP(0) STEP(1) STEP(2) STEP(3)
#undef STEP
      SCORE(rev, nst - 4 * rev)
      { const unsigned t0_ = __builtin_amdgcn_readfirstlane(*(volatile LAS unsigned*)(lds + L_T0)); tstart = t0_ ? t0_ : tstart; }
      left -= 4;
#define XREV(e) if constexpr ((e) < NXR) { if (rev == NXR + (e)) {                                   \
        _Pragma("unroll") for (int g = 0; g < 4; ++g) { PIN4(ring + (4 * (1 + (e)) + g) * 4); QK(g, 4 * (1 + (e)) + g) } \
        SCORE(e, 4) } }
      XREV(0) XREV(1)
#undef XREV
    }
  }
  {
    m = xrow_max(row16_max(m));
    float l = 0.f;
#pragma unroll
    for (int rev = 0; rev < NREVMAX; ++rev) {
      const float t = rev < NREVMAX - 3 || rev < nrev ? sl[rev * 64] : -INFINITY;
      const float p = __builtin_amdgcn_exp2f(t - m);
      l += p;
      sl[rev * 64] = p;
    }
    l = wave_sum(l);
    gst(mkrsrc(ws, WS_END), lane == 0 ? 0 : WS_END, WS_ML + (b * NH + h) * 256 + (4 * s + w) * 16, m, l, lds[L_TAG]);
    if (lane == 0) __hip_atomic_fetch_add((LAS unsigned*)(lds + L_MID), 1u, __ATOMIC_RELAXED, __HIP_MEMORY_SCOPE_WORKGROUP);
    SB();
  }
  f32x2 acc[4];
#pragma unroll
  for (int k = 0; k < 4; ++k) acc[k] = f32x2{0.f, 0.f};
#define PV1(g, i) { const float pb = dpp<BCAST + 4 * (g) + (i)>(p); const u32x4 vv = ring[(g) * 4 + (i)];  \
      _Pragma("unroll") for (int k = 0; k < 4; ++k)                                                  \
        acc[k] = __builtin_elementwise_fma(f32x2{pb, pb}, f32x2{blo(vv[k]), bhi(vv[k])}, acc[k]); }
#define PV(g) PV1(g, 0) PV1(g, 1) PV1(g, 2) PV1(g, 3) PIN4(acc);
  int left = nst - 4;
  float pn = sl[0];
#pragma nounroll
  for (int rev = 1; rev < nrev; ++rev) {
    const float p = pn;
    pn = sl[rev * 64];
#define STEP(g) { const int o_ = left > (g) ? SO : lim; PV(g) if (left > (g)) PACE() FILL(rv, g, o_) ADV() }
    STEP(0) STEP(1) STEP(2) STEP(3)
#undef STEP
    left -= 4;
  }
  {
    const float p = pn;
    PV(0)
    if (left > -3) { PV(1) }
    if (left > -2) { PV(2) }
    if (left > -1) { PV(3) }
  }
  {
    const float a0 = t32_sum(acc[0][0], acc[2][0]), a1 = t32_sum(acc[0][1], acc[2][1]);
    const float a2 = t32_sum(acc[1][0], acc[3][0]), a3 = t32_sum(acc[1][1], acc[3][1]);
    const float t0 = t16_sum(a0, a2), t1 = t16_sum(a1, a3);
    const unsigned hi = pack2(t0, t1), lo = pack2(t0 - blo(hi), t1 - bhi(hi));
    unsigned* pa = lds + L_A + ((c >> 2) * 64 + (c & 3) * 16 + 2 * w) * 4 + r;
    pa[0] = hi; pa[4] = lo;
  }
  BARRIER();
  if (cons) {
    const int ci = ((bid & 7) >> 1) * 16 + j, cb = ci >> 5, cg = ci & 31;
    const int t = tid - 256, pc = t & 7, hh = t >> 3;
    const unsigned tag = lds[L_TAG];
    const rsrc_t rw = mkrsrc(ws, WS_END);
    const int sy = WS_Y + (cb * 32 + cg) * (128 * 128);
    int vy = hh * 512 + pc * 16;
    int vf = hh < NFW ? WS_F + ((cb * 32 + cg) * NFW + hh) * 128 + pc * 16 : sy + vy;
    int vx = ci == 0 ? WS_ML + NH * 256 + (t & 127) * 64 : vf;
    const unsigned xv = *(const unsigned*)(x + cb * DM + cg * 16 + 2 * pc);
    float k0 = 0.f, k1 = 0.f, g0 = 0.f, g1 = 0.f, a0 = 0.f;
    bool done = false;
    u32x4 ya[5], yb[5];
    unsigned yat, ybt;
#define SWEEP(Y) { _Pragma("unroll") for (int i = 0; i < 4; ++i) Y[i] = gld(rw, vy + i * 128, sy);    \
      Y[4] = gld(rw, vf, 0); Y##t = __builtin_amdgcn_raw_buffer_load_b32(rw, vx, 4, 16); CFENCE(); SB(); }
#define CHECK(Y) {                                                                                   \
      unsigned bad = Y##t ^ tag;                                                                     \
      _Pragma("unroll") for (int i = 0; i < 5; ++i) bad |= (Y[i][1] ^ tag) | (Y[i][3] ^ tag);        \
      const bool take = !done && bad == 0u;                                                          \
      k0 = take ? (__uint_as_float(Y[0][0]) + __uint_as_float(Y[1][0])) + (__uint_as_float(Y[2][0]) + __uint_as_float(Y[3][0])) : k0; \
      k1 = take ? (__uint_as_float(Y[0][2]) + __uint_as_float(Y[1][2])) + (__uint_as_float(Y[2][2]) + __uint_as_float(Y[3][2])) : k1; \
      g0 = take && hh < NFW ? __uint_as_float(Y[4][0]) : g0; g1 = take && hh < NFW ? __uint_as_float(Y[4][2]) : g1; \
      done = done || take;                                                                           \
      vy = done ? WS_END : vy; vf = done ? WS_END : vf; vx = done ? WS_END : vx;                     \
      a0 = t32_sum(k0 + dpp<XOR8>(k0), g0 + dpp<XOR8>(g0));        \
      a0 = t16_sum(a0, t32_sum(k1 + dpp<XOR8>(k1), g1 + dpp<XOR8>(g1)));     \
      if (__builtin_amdgcn_ballot_w64(!done) == 0ull) break;                                         \
      CFENCE(); }
    SWEEP(ya)
    __builtin_amdgcn_s_sleep(12);
    for (unsigned spin = 0; spin < SPIN_CAP; ++spin) { SWEEP(yb) CHECK(ya) SWEEP(ya) CHECK(yb) }
#undef SWEEP
#undef CHECK
    if (c < 8) lds[L_RED + w * 32 + (r >> 1) * 16 + 2 * c + (r & 1)] = __float_as_uint(a0);
    LGKM0();
    unsigned tk = 0;
    if (lane == 0) tk = __hip_atomic_fetch_add((LAS unsigned*)(lds + L_CNT), 16u, __ATOMIC_RELAXED, __HIP_MEMORY_SCOPE_WORKGROUP);
    if (__builtin_amdgcn_readfirstlane(tk) >= 48u) {
      CFENCE();
      if (lane < 8) {
        const float* red = (const float*)lds + L_RED + 2 * lane;
        const f32x2 A = (*(const f32x2*)(red) + *(const f32x2*)(red + 32)) + (*(const f32x2*)(red + 64) + *(const f32x2*)(red + 96));
        const f32x2 F = (*(const f32x2*)(red + 16) + *(const f32x2*)(red + 48)) + (*(const f32x2*)(red + 80) + *(const f32x2*)(red + 112));
        *(unsigned*)(out + cb * DM + cg * 16 + 2 * lane) = pack2(rbf(blo(xv) + rbf(A[0])) + rbf(F[0]), rbf(bhi(xv) + rbf(A[1])) + rbf(F[1]));
      }
      if (ci == 0 && lane == 0) __builtin_amdgcn_raw_buffer_store_b32(tag, rw, WS_E, 0, 16);
    }
    asm volatile("" ::"v"(ya[0]), "v"(ya[1]), "v"(ya[2]), "v"(ya[3]), "v"(ya[4]), "v"(yat), "v"(yb[0]), "v"(yb[1]), "v"(yb[2]), "v"(yb[3]),
                 "v"(yb[4]), "v"(ybt) : "memory");
  }
}

}

extern "C" void kernel_launch(void* const* d_in, const int* in_sizes, int n_in, void* d_out, int out_size, void* d_ws,
                              size_t ws_size, hipStream_t stream) {
  attn_kernel<<<NWG, NTHR, 0, stream>>>((const u16*)d_in[1], (const u16*)d_in[2], (const u16*)d_in[0], (const u16*)d_in[3], (unsigned*)d_ws,
                                        (u16*)d_out, (const u16*)d_in[4], (const u16*)d_in[8], (const u16*)d_in[6], (const u16*)d_in[7]);
}
```

```cpp
#include <hip/hip_runtime.h>
#include <hip/hip_bf16.h>
#include <cstdint>

namespace {

constexpr int NB = 2, DM = 512, NH = 32, SK = 16384, DH = 128, FF = 1792;
constexpr int NSPLIT = 4;
constexpr int NTHR = 512;
constexpr int FSH = 2;
constexpr int NXR = 2;
constexpr int T1 = 3815, T2 = 7530, AH_FFN1 = 300, AH_FFN2 = 100, AH_CONS = 80;
constexpr int WQ_INFL = 8;
constexpr int NWG = NB * NH * NSPLIT;
constexpr int STEPK = 64;
constexpr int NSTEP = SK / NSPLIT / STEPK;
constexpr int NREVMAX = (NSTEP + FSH + 3) / 4;
constexpr int FSL = 64, NFW = FF / FSL;

constexpr int WS_E = 0;
constexpr int WS_T = 1024;
constexpr int WS_ML = 4096;
constexpr int WS_F = WS_ML + NB * NH * 256;
constexpr int WS_Y = WS_F + NB * 32 * NFW * 128;
constexpr int WS_END = WS_Y + NB * 32 * 128 * 128;
constexpr unsigned SPIN_CAP = 1u << 20;

typedef unsigned u32x4 __attribute__((ext_vector_type(4)));
typedef float f32x4 __attribute__((ext_vector_type(4)));
typedef float f32x2 __attribute__((ext_vector_type(2)));
typedef __bf16 bf16x2 __attribute__((ext_vector_type(2)));
typedef __bf16 bf16x8 __attribute__((ext_vector_type(8)));
typedef unsigned short u16;
#define GAS __attribute__((address_space(1)))
#define LAS __attribute__((address_space(3)))

__device__ __forceinline__ float dot2(unsigned a, unsigned b, float acc) {
  return __builtin_amdgcn_fdot2_f32_bf16(__builtin_bit_cast(bf16x2, a), __builtin_bit_cast(bf16x2, b), acc, false);
}
__device__ __forceinline__ float blo(unsigned u) { return __builtin_bit_cast(float, u << 16); }
__device__ __forceinline__ float bhi(unsigned u) { return __builtin_bit_cast(float, u & 0xffff0000u); }
__device__ __forceinline__ float rbf(float f) { return (float)(__bf16)f; }
__device__ __forceinline__ unsigned pack2(float lo, float hi) {
  bf16x2 v; v.x = (__bf16)lo; v.y = (__bf16)hi; return __builtin_bit_cast(unsigned, v);
}
template <int CTRL> __device__ __forceinline__ float dpp(float x) {
  return __builtin_bit_cast(float, __builtin_amdgcn_mov_dpp(__builtin_bit_cast(int, x), CTRL, 0xf, 0xf, true));
}
constexpr int XOR1 = 0xB1, XOR2 = 0x4E, XOR7 = 0x141, XOR8 = 0x128, BCAST = 0x150;
__device__ __forceinline__ float row16_sum(float x) {
  x += dpp<XOR1>(x); x += dpp<XOR2>(x); x += dpp<XOR7>(x); x += dpp<XOR8>(x); return x;
}
__device__ __forceinline__ float row16_max(float x) {
  x = fmaxf(x, dpp<XOR1>(x)); x = fmaxf(x, dpp<XOR2>(x)); x = fmaxf(x, dpp<XOR7>(x)); return fmaxf(x, dpp<XOR8>(x));
}
__device__ __forceinline__ float t16_sum(float a, float b) {
  auto s = __builtin_amdgcn_permlane16_swap(__float_as_uint(a), __float_as_uint(b), false, false);
  return __uint_as_float(s[0]) + __uint_as_float(s[1]);
}
__device__ __forceinline__ float t32_sum(float a, float b) {
  auto s = __builtin_amdgcn_permlane32_swap(__float_as_uint(a), __float_as_uint(b), false, false);
  return __uint_as_float(s[0]) + __uint_as_float(s[1]);
}
__device__ __forceinline__ float xrow_sum(float x) { x = t16_sum(x, x); return t32_sum(x, x); }
__device__ __forceinline__ float xrow_max(float x) {
  auto s = __builtin_amdgcn_permlane16_swap(__float_as_uint(x), __float_as_uint(x), false, false);
  x = fmaxf(__uint_as_float(s[0]), __uint_as_float(s[1]));
  auto t = __builtin_amdgcn_permlane32_swap(__float_as_uint(x), __float_as_uint(x), false, false);
  return fmaxf(__uint_as_float(t[0]), __uint_as_float(t[1]));
}
__device__ __forceinline__ float wave_sum(float x) { return xrow_sum(row16_sum(x)); }
typedef __amdgpu_buffer_rsrc_t rsrc_t;
__device__ __forceinline__ rsrc_t mkrsrc(const void* p, int bytes) { return __builtin_amdgcn_make_buffer_rsrc((void*)p, 0, bytes, 0x00020000); }
__device__ __forceinline__ u32x4 bld(rsrc_t r, int voff, int soff) { return __builtin_amdgcn_raw_buffer_load_b128(r, voff, soff, 2); }
__device__ __forceinline__ u32x4 bldc(rsrc_t r, int voff, int soff) { return __builtin_amdgcn_raw_buffer_load_b128(r, voff, soff, 0); }
__device__ __forceinline__ u32x4 gld(rsrc_t r, int voff, int soff) { return __builtin_amdgcn_raw_buffer_load_b128(r, voff, soff, 16); }
__device__ __forceinline__ void gst(rsrc_t r, int voff, int soff, float v0, float v1, unsigned tag) {
  __builtin_amdgcn_raw_buffer_store_b128(u32x4{__float_as_uint(v0), tag, __float_as_uint(v1), tag}, r, voff, soff, 16);
}
#define SB() __builtin_amdgcn_sched_barrier(0)
__device__ __forceinline__ unsigned load_tag(const unsigned* ws) {
  unsigned e;
  asm volatile("s_load_dword %0, %1, 0x0\n\ts_waitcnt lgkmcnt(0)" : "=s"(e) : "s"(ws) : "memory");
  return e + 1u;
}
#define PIN4(a) asm volatile("" : "+v"((a)[0]), "+v"((a)[1]), "+v"((a)[2]), "+v"((a)[3])::"memory"); SB()
#define LGKM0() asm volatile("s_waitcnt lgkmcnt(0)" ::: "memory")
#define CFENCE() asm volatile("" ::: "memory")
#define BARRIER() do { LGKM0(); __builtin_amdgcn_s_barrier(); CFENCE(); } while (0)
__device__ __forceinline__ f32x4 mfma(u32x4 a, u32x4 b, f32x4 c) {
  return __builtin_amdgcn_mfma_f32_16x16x32_bf16(__builtin_bit_cast(bf16x8, a), __builtin_bit_cast(bf16x8, b), c, 0, 0, 0);
}
__device__ __forceinline__ u32x4 bfrag(const u32x4* w, int e) {
  u32x4 o;
#pragma unroll
  for (int jj = 0; jj < 4; ++jj) o[jj] = __builtin_amdgcn_perm(w[2 * jj + 1][e >> 1], w[2 * jj][e >> 1], (e & 1) ? 0x07060302u : 0x05040100u);
  return o;
}
__device__ __forceinline__ u32x4 layernorm8(u32x4 xa) {
  float e[8];
#pragma unroll
  for (int k = 0; k < 4; ++k) { e[2 * k] = blo(xa[k]); e[2 * k + 1] = bhi(xa[k]); }
  const float mean = wave_sum(((e[0] + e[1]) + (e[2] + e[3])) + ((e[4] + e[5]) + (e[6] + e[7]))) * (1.0f / DM);
  float q = 0.f;
#pragma unroll
  for (int k = 0; k < 8; ++k) { const float d = e[k] - mean; q = fmaf(d, d, q); }
  const float var = wave_sum(q) * (1.0f / DM);
  const float mb = rbf(mean), sd = rbf(sqrtf(rbf(rbf(var) + 1e-5f)));
  const float ri = 1.0f / sd;
  u32x4 o;
#pragma unroll
  for (int k = 0; k < 4; ++k) {
    const float d0 = rbf(e[2 * k] - mb), d1 = rbf(e[2 * k + 1] - mb);
    float q0 = d0 * ri, q1 = d1 * ri;
    q0 = fmaf(fmaf(-q0, sd, d0), ri, q0); q1 = fmaf(fmaf(-q1, sd, d1), ri, q1);
    o[k] = pack2(q0, q1);
  }
  return o;
}

constexpr int L_QP = 0;
constexpr int L_A = L_QP + 4 * DH;
constexpr int L_CNT = L_A + 1024, L_TAG = L_CNT + 1, L_MID = L_CNT + 2, L_T0 = L_CNT + 3, L_FW = L_CNT + 4;
constexpr int L_RED = L_CNT + 16;
constexpr int L_XN = L_RED + 128;
constexpr int L_GU = L_XN + 4 * 128;
constexpr int L_H = L_GU + 4 * 256;
constexpr int L_S = L_H + 4 * 64;
constexpr int L_TOT = L_S + 4 * NREVMAX * 64;

__global__ void __launch_bounds__(NTHR, 2) attn_kernel(const u16* __restrict__ kc, const u16* __restrict__ vc, const u16* __restrict__ x,
                                                       const u16* __restrict__ wq, unsigned* ws, u16* __restrict__ out,
                                                       const u16* __restrict__ wo, const u16* __restrict__ w2,
                                                       const u16* __restrict__ w1, const u16* __restrict__ w3) {
  __shared__ __attribute__((aligned(16))) unsigned lds[L_TOT];
  const unsigned long long tb0 = __builtin_amdgcn_s_memrealtime();
  SB();
  const int bid = blockIdx.x;
  const int tid = threadIdx.x, lane = tid & 63, wv = __builtin_amdgcn_readfirstlane(tid >> 6);
  const int j = bid >> 3, h = (bid & 7) * 4 + (j >> 3), b = (j >> 2) & 1, s = j & 3;
  const int r = lane >> 4, c = lane & 15;
  const int w = wv - 4;
  const int so0 = s * NSTEP + (((2 * s + b) * 8 + 12 * (j >> 3)) & 63);
  const int kvb = (b * NH + h) * (SK * DH * 2);
  const int vo = lane * 16 + (w & 1) * 8192 + (w >> 1) * 32768;
  constexpr int lim = SK * DH * 2;
  const rsrc_t rk = mkrsrc((const char*)kc + (unsigned)kvb, lim);
  u32x4 ring[16 * (1 + NXR)];
#define FILL(rs, g, o) _Pragma("unroll") for (int i = 0; i < 4; ++i) { ring[(g) * 4 + i] = bld(rs, vo + i * 1024, o); SB(); }
  if (wv >= 4) { FILL(rk, 4, so0 << 14) FILL(rk, 5, (so0 << 14) + 4096) }
  int wu = wv;
  asm volatile("" : "+s"(wu) : "s"(tb0) : "memory");
  const unsigned tb = (unsigned)tb0 | 1u;
  const bool fgrp = (bid & 1) == 0 && (j >> 2) < 7;
  const int fs = fgrp ? FSH : 0;
  const int nst = NSTEP + (s == 0 ? -3 * fs : fs), st0 = s * NSTEP + (s == 0 ? 0 : (s - 4) * fs);

  if (wu < 4) {
    const int a = wv;
    __builtin_amdgcn_s_setprio(3);
    const rsrc_t rx = mkrsrc(x, NB * DM * 2);
    const u32x4 xa = bldc(rx, lane * 16, b * (DM * 2));
    u32x4 wr[32];
    const rsrc_t rq = mkrsrc(wq + h * DH, (DM * NH * DH - h * DH) * 2);
    const int vq = r * (8 * NH * DH * 2) + c * 16;
#define LD_WQ(i) { if ((i) >= WQ_INFL) asm volatile("s_waitcnt vmcnt(%0)" ::"n"(WQ_INFL - 1) : "memory"); \
      wr[i] = bldc(rq, vq, (128 * a + 32 * ((((i) >> 3) + s) & 3) + ((i) & 7)) * (NH * DH * 2)); SB(); }
#pragma unroll
    for (int i = 0; i < WQ_INFL; ++i) LD_WQ(i)
    if (a == 0) {
      const unsigned t = load_tag(ws);
      if (lane == 0) { lds[L_CNT] = 0u; lds[L_TAG] = t; lds[L_MID] = 0u; lds[L_T0] = 0u; }
      if (bid == 0) gst(mkrsrc(ws, WS_END), lane < 8 ? WS_T + lane * 128 : WS_END, 0, __uint_as_float(tb), __uint_as_float(tb), t);
    }
    SB();
    unsigned* xn = lds + L_XN + a * 128;
    {
      const u32x4 n0 = layernorm8(xa);
      if (r == a) *(u32x4*)(xn + 64 * b + 4 * c) = n0;
    }
    CFENCE(); SB();
    f32x4 d[8];
#pragma unroll
    for (int e = 0; e < 8; ++e) d[e] = f32x4{0.f, 0.f, 0.f, 0.f};
#pragma unroll
    for (int ks = 0; ks < 4; ++ks) {
#pragma unroll
      for (int i = 8 * ks + WQ_INFL; i < 8 * ks + 8 + WQ_INFL; ++i) if (i < 32) LD_WQ(i)
      const u32x4 af = *(const u32x4*)(xn + 64 * b + 16 * ((ks + s) & 3) + 4 * r);
#pragma unroll
      for (int e = 0; e < 8; ++e) d[e] = mfma(af, bfrag(wr + 8 * ks, e), d[e]);
      SB();
    }
#undef LD_WQ
    if (r == 0) {
      *(f32x4*)(lds + L_QP + a * DH + 8 * c) = f32x4{d[0][0], d[1][0], d[2][0], d[3][0]};
      *(f32x4*)(lds + L_QP + a * DH + 8 * c + 4) = f32x4{d[4][0], d[5][0], d[6][0], d[7][0]};
    }
    BARRIER();
    const unsigned tag = lds[L_TAG];
    const rsrc_t rw = mkrsrc(ws, WS_END);
    if (a == 1) {
      const u32x4 g = gld(rw, WS_T + (bid & 7) * 128, 0);
      if (g[1] == tag && tb - g[0] < 200u) *(volatile LAS unsigned*)(lds + L_T0) = g[0];
    }
    u32x4 bf[32];
    {
      const rsrc_t ro = mkrsrc(wo + (size_t)h * DH * DM, DH * DM * 2);
#pragma unroll
      for (int ks = 0; ks < 4; ++ks) {
#pragma unroll
        for (int i = 0; i < 8; ++i) {
          __builtin_amdgcn_s_sleep(8);
          wr[i] = bldc(ro, r * 8192 + c * 16 + (i & 3) * 1024, ks * 32768 + (i >> 2) * 4096 + a * 256);
          SB();
        }
#pragma unroll
        for (int e = 0; e < 8; ++e) { bf[8 * ks + e] = bfrag(wr, e); asm volatile("" : "+v"(bf[8 * ks + e])); }
        SB();
      }
    }
    if (fgrp && s == 0) {
      const int sl = ((bid & 7) >> 1) * 7 + (j >> 2), f0 = sl * FSL;
      const u32x4 xo = bldc(rx, lane * 16, (b ^ 1) * (DM * 2));
      const GAS u32x4* p13 = (const GAS u32x4*)((c < 8 ? w1 : w3) + (size_t)(128 * a + 8 * r) * FF + f0 + 8 * (c & 7));
#pragma unroll
      for (int i = 0; i < 8; ++i) wr[i] = __builtin_nontemporal_load(p13 + i * (FF / 8));
      SB();
      {
        const u32x4 n1 = layernorm8(xo);
        if (r == a) *(u32x4*)(xn + 64 * (b ^ 1) + 4 * c) = n1;
      }
      CFENCE();
#pragma unroll
      for (int e = 0; e < 8; ++e) d[e] = f32x4{0.f, 0.f, 0.f, 0.f};
      const rsrc_t r2 = mkrsrc(w2 + (size_t)f0 * DM, FSL * DM * 2);
#pragma unroll
      for (int ks = 0; ks < 4; ++ks) {
        const u32x4 af = *(const u32x4*)(xn + 64 * (c & 1) + 16 * ks + 4 * r);
#pragma unroll
        for (int e = 0; e < 8; ++e) d[e] = mfma(af, bfrag(wr, e), d[e]);
        SB();
#pragma unroll
        for (int jj = 0; jj < 8; ++jj)
          wr[jj] = ks < 3 ? __builtin_nontemporal_load(p13 + (32 * (ks + 1) + jj) * (FF / 8))
                          : bld(r2, r * 8192 + c * 16 + (jj & 3) * 1024, (jj >> 2) * 4096 + a * 256);
        SB();
      }
      if (r < 2) {
        float* gu = (float*)lds + L_GU + a * 256 + r * 128 + 8 * c;
        *(f32x4*)(gu) = r ? f32x4{d[0][1], d[1][1], d[2][1], d[3][1]} : f32x4{d[0][0], d[1][0], d[2][0], d[3][0]};
        *(f32x4*)(gu + 4) = r ? f32x4{d[4][1], d[5][1], d[6][1], d[7][1]} : f32x4{d[4][0], d[5][0], d[6][0], d[7][0]};
      }
      LGKM0();
      if (lane == 0) __hip_atomic_fetch_add((LAS unsigned*)(lds + L_CNT), 1u, __ATOMIC_RELAXED, __HIP_MEMORY_SCOPE_WORKGROUP);
      while (*(volatile LAS unsigned*)(lds + L_CNT) < 4u) __builtin_amdgcn_s_sleep(1);
      CFENCE();
      {
        const float* gu = (const float*)lds + L_GU + (lane >> 5) * 128 + 2 * (lane & 31);
        f32x2 g = {0.f, 0.f}, u = {0.f, 0.f};
#pragma unroll
        for (int aa = 0; aa < 4; ++aa) { g += *(const f32x2*)(gu + aa * 256); u += *(const f32x2*)(gu + aa * 256 + 64); }
        float hv[2];
#pragma unroll
        for (int k = 0; k < 2; ++k) { const float gg = rbf(g[k]); hv[k] = rbf(gg / (1.0f + __expf(-gg))) * rbf(u[k]); }
        lds[L_H + a * 64 + lane] = pack2(hv[0], hv[1]);
      }
      CFENCE();
#pragma unroll
      for (int e = 0; e < 8; ++e) d[e] = f32x4{0.f, 0.f, 0.f, 0.f};
#pragma unroll
      for (int ks = 0; ks < 2; ++ks) {
        const u32x4 af = *(const u32x4*)(lds + L_H + a * 64 + 32 * (c & 1) + 16 * ks + 4 * r);
#pragma unroll
        for (int e = 0; e < 8; ++e) d[e] = mfma(af, bfrag(wr, e), d[e]);
        SB();
        if (ks == 0) {
#pragma unroll
          for (int jj = 0; jj < 8; ++jj) wr[jj] = bld(r2, r * 8192 + c * 16 + (jj & 3) * 1024, 32768 + (jj >> 2) * 4096 + a * 256);
          SB();
        }
      }
      {
        const int fo = WS_F + (((r & 1) * 32 + 8 * a + (c >> 1)) * NFW + sl) * 128 + (c & 1) * 64 + (r >> 1) * 32;
        float t[4];
#pragma unroll
        for (int e = 0; e < 4; ++e) {
          const float p0 = (r & 1) ? d[e][1] : d[e][0], p1 = (r & 1) ? d[4 + e][1] : d[4 + e][0];
          t[e] = r < 2 ? p0 : p1;
        }
        gst(rw, fo, 0, t[0], t[1], tag); gst(rw, fo + 16, 0, t[2], t[3], tag);
      }
      SB();
    }
    if (a == 0) {
      while (*(volatile LAS unsigned*)(lds + L_MID) < 4u) __builtin_amdgcn_s_sleep(16);
      if (!(fgrp && s == 0)) for (int i = 0; i < 3; ++i) __builtin_amdgcn_s_sleep(127);
      u32x4 ml;
      for (unsigned spin = 0; spin < SPIN_CAP; ++spin) {
        CFENCE();
        ml = gld(rw, c * 16, WS_ML + (b * NH + h) * 256);
        if (__builtin_amdgcn_ballot_w64(((ml[1] ^ tag) | (ml[3] ^ tag)) != 0u) == 0ull) break;
        __builtin_amdgcn_s_sleep(64);
      }
      const float mw = __uint_as_float(ml[0]), M = row16_max(mw);
      const float e = __builtin_amdgcn_exp2f(mw - M);
      const float f = e / row16_sum(e * __uint_as_float(ml[2]));
      if ((lane >> 2) == s) lds[L_FW + (lane & 3)] = __float_as_uint(f);
    }
    BARRIER();
    u32x4 af[4];
#pragma unroll
    for (int ks = 0; ks < 4; ++ks) af[ks] = *(const u32x4*)(lds + L_A + (ks * 64 + r * 16 + (c & 7)) * 4);
    const f32x2 fw = *(const f32x2*)(lds + L_FW + 2 * (r & 1));
    const float f0 = fw[0], f1 = fw[1];
#pragma unroll
    for (int e = 0; e < 8; ++e) d[e] = f32x4{0.f, 0.f, 0.f, 0.f};
#pragma unroll
    for (int ks = 0; ks < 4; ++ks)
#pragma unroll
      for (int e = 0; e < 8; ++e) d[e] = mfma(af[ks], bf[8 * ks + e], d[e]);
    float v[4];
#pragma unroll
    for (int e = 0; e < 4; ++e) {
      const float p0 = d[e][0] + d[e][1], p1 = d[e][2] + d[e][3], q0 = d[4 + e][0] + d[4 + e][1], q1 = d[4 + e][2] + d[4 + e][3];
      v[e] = f0 * (r < 2 ? p0 : q0) + f1 * (r < 2 ? p1 : q1);
    }
    gst(rw, (c >> 1) * (128 * 128) + (c & 1) * 64 + r * 16, WS_Y + ((b * 32 + 8 * a) * 128 + h * NSPLIT + s) * 128,
        t16_sum(v[0], v[2]), t16_sum(v[1], v[3]), tag);
    return;
  }

  const int send = st0 + nst;
  const rsrc_t rv = mkrsrc((const char*)vc + (unsigned)kvb, lim);
  int so = so0 + 1;
  const bool ffn = fgrp && s == 0, cons = (bid & 1) == 0 && j < 16;
  unsigned tstart = tb;
  constexpr int NF = NSTEP - 3 * FSH, NS = NSTEP + FSH;
  int dt = ffn ? T1 * 64 / NF : fgrp ? T1 * 64 / NS : T1 * 64 / NSTEP;
  int tg = dt * 4 * (1 + NXR) - (ffn ? AH_FFN1 * 64 : 0);
#define PACE() { while ((unsigned)__builtin_amdgcn_s_memrealtime() - tstart < (unsigned)(tg >> 6)) __builtin_amdgcn_s_sleep(1); tg += dt; }
#define ADV() ++so; so = so >= send ? st0 : so;
#define SO (((so >> 2) << 16) | ((so & 1) << 12) | ((so & 2) << 13))
  ADV()
#pragma unroll
  for (int g = 6; g < 4 + 4 * NXR; ++g) { FILL(rk, g, SO) ADV() }
#pragma unroll
  for (int g = 0; g < 4; ++g) { FILL(rk, g, SO) ADV() }
  BARRIER();
  unsigned qp[4];
  {
    f32x4 t0 = *(const f32x4*)(lds + L_QP + 8 * c), t1 = *(const f32x4*)(lds + L_QP + 8 * c + 4);
#pragma unroll
    for (int aa = 1; aa < 4; ++aa) {
      t0 += *(const f32x4*)(lds + L_QP + aa * DH + 8 * c);
      t1 += *(const f32x4*)(lds + L_QP + aa * DH + 8 * c + 4);
    }
    qp[0] = pack2(t0[0], t0[1]); qp[1] = pack2(t0[2], t0[3]); qp[2] = pack2(t1[0], t1[1]); qp[3] = pack2(t1[2], t1[3]);
  }
  constexpr float SCALE = 0.088388347648318440f, LOG2E = 1.4426950408889634f;
  float* sl = (float*)lds + L_S + w * (NREVMAX * 64) + lane;
  float m = -INFINITY;
  float xs[16];
#define QK(g, slot)                                                                                  \
    _Pragma("unroll") for (int i = 0; i < 4; ++i) {                                                  \
      float d_ = 0.f;                                                                                \
      _Pragma("unroll") for (int k = 0; k < 4; ++k) d_ = dot2(ring[(slot) * 4 + i][k], qp[k], d_);   \
      xs[(g) * 4 + i] = d_;                                                                          \
    }                                                                                                \
    PIN4(xs + (g) * 4);
#define TR(HALF, CTRL) _Pragma("unroll") for (int k = 0; k < (HALF); ++k) {                          \
      const float t1_ = xs[k] + dpp<CTRL>(xs[k]), t2_ = xs[k + (HALF)] + dpp<CTRL>(xs[k + (HALF)]);  \
      xs[k] = (c & (HALF)) ? t2_ : t1_; }
#define SCORE(rev, nvalid)                                                                           \
  {                                                                                                  \
    TR(8, XOR8) TR(4, XOR7) TR(2, XOR2) TR(1, XOR1)                                                  \
    float t = rbf(rbf(xs[0]) * SCALE) * LOG2E;                     \
    t = (c >> 2) < (nvalid) ? t : -INFINITY;                                                         \
    m = fmaxf(m, t);                                                                                 \
    sl[(rev) * 64] = t;                                                                              \
    SB();                                                                                            \
  }
  const int nrev = (nst + 3) >> 2;
  {
    int left = nst - 4 * (1 + NXR);
    rsrc_t rn = rk;
#pragma nounroll
    for (int rev = NXR; rev < nrev; ++rev) {
      if (rev == nrev - 1) {
        rn = rv; so = so0; left = 4;
        tg = T1 * 64;
        dt = ffn ? (T2 - T1 - AH_FFN2) * 64 / NF : cons ? (T2 - T1 - AH_CONS) * 64 / NS : fgrp ? (T2 - T1) * 64 / NS : (T2 - T1) * 64 / NSTEP;
      }
#define STEP(g) { const int o_ = left > (g) ? SO : lim; QK(g, g) if (left > (g)) PACE() FILL(rn, g, o_) ADV() }
      STEP(0) STEP(1) STEP(2) STEP(3)
#undef STEP
      SCORE(rev, nst - 4 * rev)
      { const unsigned t0_ = __builtin_amdgcn_readfirstlane(*(volatile LAS unsigned*)(lds + L_T0)); tstart = t0_ ? t0_ : tstart; }
      left -= 4;
#define XREV(e) if constexpr ((e) < NXR) { if (rev == NXR + (e)) {                                   \
        _Pragma("unroll") for (int g = 0; g < 4; ++g) { PIN4(ring + (4 * (1 + (e)) + g) * 4); QK(g, 4 * (1 + (e)) + g) } \
        SCORE(e, 4) } }
      XREV(0) XREV(1)
#undef XREV
    }
  }
  {
    m = xrow_max(row16_max(m));
    float l = 0.f;
#pragma unroll
    for (int rev = 0; rev < NREVMAX; ++rev) {
      const float t = rev < NREVMAX - 3 || rev < nrev ? sl[rev * 64] : -INFINITY;
      const float p = __builtin_amdgcn_exp2f(t - m);
      l += p;
      sl[rev * 64] = p;
    }
    l = wave_sum(l);
    gst(mkrsrc(ws, WS_END), lane == 0 ? 0 : WS_END, WS_ML + (b * NH + h) * 256 + (4 * s + w) * 16, m, l, lds[L_TAG]);
    if (lane == 0) __hip_atomic_fetch_add((LAS unsigned*)(lds + L_MID), 1u, __ATOMIC_RELAXED, __HIP_MEMORY_SCOPE_WORKGROUP);
    SB();
  }
  f32x2 acc[4];
#pragma unroll
  for (int k = 0; k < 4; ++k) acc[k] = f32x2{0.f, 0.f};
#define PV1(g, i) { const float pb = dpp<BCAST + 4 * (g) + (i)>(p); const u32x4 vv = ring[(g) * 4 + (i)];  \
      _Pragma("unroll") for (int k = 0; k < 4; ++k)                                                  \
        acc[k] = __builtin_elementwise_fma(f32x2{pb, pb}, f32x2{blo(vv[k]), bhi(vv[k])}, acc[k]); }
#define PV(g) PV1(g, 0) PV1(g, 1) PV1(g, 2) PV1(g, 3) PIN4(acc);
  int left = nst - 4;
  float pn = sl[0];
#pragma nounroll
  for (int rev = 1; rev < nrev; ++rev) {
    const float p = pn;
    pn = sl[rev * 64];
#define STEP(g) { const int o_ = left > (g) ? SO : lim; PV(g) if (left > (g) + 2) PACE() FILL(rv, g, o_) ADV() }
    STEP(0) STEP(1) STEP(2) STEP(3)
#undef STEP
    left -= 4;
  }
  {
    const float p = pn;
    PV(0)
    if (left > -3) { PV(1) }
    if (left > -2) { PV(2) }
    if (left > -1) { PV(3) }
  }
  {
    const float a0 = t32_sum(acc[0][0], acc[2][0]), a1 = t32_sum(acc[0][1], acc[2][1]);
    const float a2 = t32_sum(acc[1][0], acc[3][0]), a3 = t32_sum(acc[1][1], acc[3][1]);
    const float t0 = t16_sum(a0, a2), t1 = t16_sum(a1, a3);
    const unsigned hi = pack2(t0, t1), lo = pack2(t0 - blo(hi), t1 - bhi(hi));
    unsigned* pa = lds + L_A + ((c >> 2) * 64 + (c & 3) * 16 + 2 * w) * 4 + r;
    pa[0] = hi; pa[4] = lo;
  }
  BARRIER();
  if (cons) {
    const int ci = ((bid & 7) >> 1) * 16 + j, cb = ci >> 5, cg = ci & 31;
    const int t = tid - 256, pc = t & 7, hh = t >> 3;
    const unsigned tag = lds[L_TAG];
    const rsrc_t rw = mkrsrc(ws, WS_END);
    const int sy = WS_Y + (cb * 32 + cg) * (128 * 128);
    int vy = hh * 512 + pc * 16;
    int vf = hh < NFW ? WS_F + ((cb * 32 + cg) * NFW + hh) * 128 + pc * 16 : sy + vy;
    int vx = ci == 0 ? WS_ML + NH * 256 + (t & 127) * 64 : vf;
    const unsigned xv = *(const unsigned*)(x + cb * DM + cg * 16 + 2 * pc);
    float k0 = 0.f, k1 = 0.f, g0 = 0.f, g1 = 0.f, a0 = 0.f;
    bool done = false;
    u32x4 ya[5], yb[5];
    unsigned yat, ybt;
#define SWEEP(Y) { _Pragma("unroll") for (int i = 0; i < 4; ++i) Y[i] = gld(rw, vy + i * 128, sy);    \
      Y[4] = gld(rw, vf, 0); Y##t = __builtin_amdgcn_raw_buffer_load_b32(rw, vx, 4, 16); CFENCE(); SB(); }
#define CHECK(Y) {                                                                                   \
      unsigned bad = Y##t ^ tag;                                                                     \
      _Pragma("unroll") for (int i = 0; i < 5; ++i) bad |= (Y[i][1] ^ tag) | (Y[i][3] ^ tag);        \
      const bool take = !done && bad == 0u;                                                          \
      k0 = take ? (__uint_as_float(Y[0][0]) + __uint_as_float(Y[1][0])) + (__uint_as_float(Y[2][0]) + __uint_as_float(Y[3][0])) : k0; \
      k1 = take ? (__uint_as_float(Y[0][2]) + __uint_as_float(Y[1][2])) + (__uint_as_float(Y[2][2]) + __uint_as_float(Y[3][2])) : k1; \
      g0 = take && hh < NFW ? __uint_as_float(Y[4][0]) : g0; g1 = take && hh < NFW ? __uint_as_float(Y[4][2]) : g1; \
      done = done || take;                                                                           \
      vy = done ? WS_END : vy; vf = done ? WS_END : vf; vx = done ? WS_END : vx;                     \
      a0 = t32_sum(k0 + dpp<XOR8>(k0), g0 + dpp<XOR8>(g0));        \
      a0 = t16_sum(a0, t32_sum(k1 + dpp<XOR8>(k1), g1 + dpp<XOR8>(g1)));     \
      if (__builtin_amdgcn_ballot_w64(!done) == 0ull) break;                                         \
      CFENCE(); }
    SWEEP(ya)
    __builtin_amdgcn_s_sleep(12);
    for (unsigned spin = 0; spin < SPIN_CAP; ++spin) { SWEEP(yb) CHECK(ya) SWEEP(ya) CHECK(yb) }
#undef SWEEP
#undef CHECK
    if (c < 8) lds[L_RED + w * 32 + (r >> 1) * 16 + 2 * c + (r & 1)] = __float_as_uint(a0);
    LGKM0();
    unsigned tk = 0;
    if (lane == 0) tk = __hip_atomic_fetch_add((LAS unsigned*)(lds + L_CNT), 16u, __ATOMIC_RELAXED, __HIP_MEMORY_SCOPE_WORKGROUP);
    if (__builtin_amdgcn_readfirstlane(tk) >= 48u) {
      CFENCE();
      if (lane < 8) {
        const float* red = (const float*)lds + L_RED + 2 * lane;
        const f32x2 A = (*(const f32x2*)(red) + *(const f32x2*)(red + 32)) + (*(const f32x2*)(red + 64) + *(const f32x2*)(red + 96));
        const f32x2 F = (*(const f32x2*)(red + 16) + *(const f32x2*)(red + 48)) + (*(const f32x2*)(red + 80) + *(const f32x2*)(red + 112));
        *(unsigned*)(out + cb * DM + cg * 16 + 2 * lane) = pack2(rbf(blo(xv) + rbf(A[0])) + rbf(F[0]), rbf(bhi(xv) + rbf(A[1])) + rbf(F[1]));
      }
      if (ci == 0 && lane == 0) __builtin_amdgcn_raw_buffer_store_b32(tag, rw, WS_E, 0, 16);
    }
    asm volatile("" ::"v"(ya[0]), "v"(ya[1]), "v"(ya[2]), "v"(ya[3]), "v"(ya[4]), "v"(yat), "v"(yb[0]), "v"(yb[1]), "v"(yb[2]), "v"(yb[3]),
                 "v"(yb[4]), "v"(ybt) : "memory");
  }
}

}

extern "C" void kernel_launch(void* const* d_in, const int* in_sizes, int n_in, void* d_out, int out_size, void* d_ws,
                              size_t ws_size, hipStream_t stream) {
  attn_kernel<<<NWG, NTHR, 0, stream>>>((const u16*)d_in[1], (const u16*)d_in[2], (const u16*)d_in[0], (const u16*)d_in[3], (unsigned*)d_ws,
                                        (u16*)d_out, (const u16*)d_in[4], (const u16*)d_in[8], (const u16*)d_in[6], (const u16*)d_in[7]);
}
```
